```python
import torch

def _softmax(x, axis=-1):
    return torch.softmax(x, dim=axis)
def _gelu(x):
    return torch.nn.functional.gelu(x, approximate="tanh")
def _silu(x):
    return torch.nn.functional.silu(x)
def _sigmoid(x):
    return torch.sigmoid(x)
def _layernorm(x, axis=-1, eps=1e-5):
    m = x.mean(dim=axis, keepdim=True)
    v = x.var(dim=axis, keepdim=True, unbiased=False)
    return (x - m) / torch.sqrt(v + eps)
def _scatter_add(tgt, idx, src, axis=0):
    return tgt.clone().index_add_(axis, idx, src)
def _segment_sum(src, seg, n):
    out = torch.zeros((n,) + src.shape[1:], dtype=src.dtype)
    return out.index_add_(0, seg, src)
def _pad(x, pw):
    flat = tuple(v for pair in reversed(pw) for v in pair)
    return torch.nn.functional.pad(x, flat)

kernel_name = 'sequential_preLN__sliding_block_cached__dense_activation_relu__decode'

def setup_inputs():

    _w = (lambda a: (lambda t: t.to(torch.bfloat16) if t.is_floating_point() else t)(torch.as_tensor(a)))
    def _A_setup():
        B, Sq, D = 8, 1, 576
        Hq, Hkv, Sk, Dh = 32, 16, 8192, 128
        assert Hq % Hkv == 0, (Hq, Hkv)
        return {
            "x": _w((lambda *s: torch.randn(*s, dtype=torch.bfloat16) * 0.05)(B, Sq, D)),
            "k_cache": _w((lambda *s: torch.randn(*s, dtype=torch.bfloat16) * 3.2)(B, Hkv, Sk, Dh)),
            "v_cache": _w((lambda *s: torch.randn(*s, dtype=torch.bfloat16) * 0.4)(B, Hkv, Sk, Dh)),
            "wq": _w((lambda *s: torch.randn(*s, dtype=torch.bfloat16) * 3.2)(D, Hq * Dh) * (D ** -0.5)),
            "wo": _w((lambda *s: torch.randn(*s, dtype=torch.bfloat16) * 0.4)(Hq * Dh, D) * ((Hq * Dh) ** -0.5)),
        }
    _A_in = _A_setup()

    def _B_setup():
        B, S, D, Dff = 8, 1, 576, 4608
        return {
            "x": _w((lambda *s: torch.randn(*s, dtype=torch.bfloat16) * 0.05)(B, S, D)),
            "w1": _w((lambda *s: torch.randn(*s, dtype=torch.bfloat16) * 0.05)(D, Dff)),
            "w2": _w((lambda *s: torch.randn(*s, dtype=torch.bfloat16) * 0.05)(Dff, D)),
        }
    _B_in = _B_setup()

    return {
        "A_x": _A_in["x"],
        "A_k_cache": _A_in["k_cache"],
        "A_v_cache": _A_in["v_cache"],
        "A_wq": _A_in["wq"],
        "A_wo": _A_in["wo"],
        "B_x": _B_in["x"],
        "B_w1": _B_in["w1"],
        "B_w2": _B_in["w2"],
    }

def reference(A_x, A_k_cache, A_v_cache, A_wq, A_wo, B_x, B_w1, B_w2):
    def _A_ref(x, k_cache, v_cache, wq, wo):
        B, Sq, D = x.shape
        Hq, Dh = 32, 128
        Hkv, Sk = k_cache.shape[1], k_cache.shape[2]
        W = min(4096, max(256, Sk // 4))
        rep = Hq // Hkv
        q = (lambda x, *d: x.permute(*d))((x @ wq).reshape(B, Sq, Hq, Dh), 0, 2, 1, 3)
        k = (lambda x, n, axis: torch.repeat_interleave(x, n, dim=axis))(k_cache[:, :, Sk - W:, :], rep, axis=1)
        v = (lambda x, n, axis: torch.repeat_interleave(x, n, dim=axis))(v_cache[:, :, Sk - W:, :], rep, axis=1)
        scores = torch.einsum("bhqd,bhkd->bhqk", q, k) * (Dh ** -0.5)
        p = _softmax(scores, axis=-1)
        o = torch.einsum("bhqk,bhkd->bhqd", p, v)
        return (lambda x, *d: x.permute(*d))(o, 0, 2, 1, 3).reshape(B, Sq, Hq * Dh) @ wo

    def _B_ref(x, w1, w2):
        h = torch.einsum("bsd,df->bsf", x, w1)
        zero = torch.zeros_like(h)
        h = torch.maximum(h, zero)
        return torch.einsum("bsf,fd->bsd", h, w2)

    x = A_x
    h = x + _A_ref(_layernorm(x, axis=-1), A_k_cache, A_v_cache, A_wq, A_wo)
    h2 = h + _B_ref(_layernorm(h, axis=-1), B_w1, B_w2)
    return h2
```

```cpp
#include <hip/hip_runtime.h>
#include <hip/hip_bf16.h>
#include <stdint.h>

typedef unsigned v4u __attribute__((ext_vector_type(4)));
typedef float f32x4 __attribute__((ext_vector_type(4)));
typedef short bf16x8 __attribute__((ext_vector_type(8)));
typedef unsigned short u16;

constexpr int B = 8, D = 576, HQ = 32, HKV = 16, SK = 8192, DH = 128, W = 2048, DFF = 4608;
#define EMU_SCORE 1

__device__ __forceinline__ float bf2f(u16 b) { return __builtin_bit_cast(float, (unsigned)b << 16); }
__device__ __forceinline__ u16 f2bf(float f) { __bf16 h = (__bf16)f; return __builtin_bit_cast(u16, h); }
__device__ __forceinline__ float rbf(float f) { return bf2f(f2bf(f)); }
__device__ __forceinline__ f32x4 mfma(v4u a, v4u b, f32x4 c) {
  return __builtin_amdgcn_mfma_f32_16x16x32_bf16(__builtin_bit_cast(bf16x8, a), __builtin_bit_cast(bf16x8, b), c, 0, 0, 0);
}
__device__ __forceinline__ float wave_sum(float v) {
#pragma unroll
  for (int m = 32; m >= 1; m >>= 1) v += __shfl_xor(v, m);
  return v;
}

__device__ __forceinline__ void ln_row(const u16* x, float* out, int lane) {
  float v[9];
  float s = 0.f;
#pragma unroll
  for (int i = 0; i < 9; ++i) { v[i] = bf2f(x[lane + 64 * i]); s += v[i]; }
  s = wave_sum(s);
  const float mean = s * (1.0f / D);
  float q = 0.f;
#pragma unroll
  for (int i = 0; i < 9; ++i) { float d = v[i] - mean; q += d * d; }
  q = wave_sum(q);
  const float var = rbf(q * (1.0f / D));
  const float mb = rbf(mean);
  const float den = rbf(sqrtf(rbf(var + 1e-5f)));
#pragma unroll
  for (int i = 0; i < 9; ++i) out[lane + 64 * i] = rbf(rbf(v[i] - mb) / den);
}

__global__ void __launch_bounds__(512) k_qproj(const u16* x, const u16* wq, u16* q) {
  __shared__ float xn[B][D];
  __shared__ float red[8][B][64];
  const int lane = threadIdx.x & 63, w = threadIdx.x >> 6;
  ln_row(x + w * D, xn[w], lane);
  __syncthreads();
  const int col = blockIdx.x * 64 + lane;
  float acc[B];
#pragma unroll
  for (int b = 0; b < B; ++b) acc[b] = 0.f;
  for (int k = w * 72; k < w * 72 + 72; ++k) {
    float wv = bf2f(wq[(size_t)k * (HQ * DH) + col]);
#pragma unroll
    for (int b = 0; b < B; ++b) acc[b] += xn[b][k] * wv;
  }
#pragma unroll
  for (int b = 0; b < B; ++b) red[w][b][lane] = acc[b];
  __syncthreads();
  float s = 0.f;
#pragma unroll
  for (int i = 0; i < 8; ++i) s += red[i][w][lane];
  q[w * (HQ * DH) + col] = f2bf(s);
}

__global__ void __launch_bounds__(512, 2) k_attn(const u16* kc, const u16* vc, const u16* q, float* part) {
  __shared__ float sm_o[8][2][DH];
  __shared__ float sm_ml[8][2][2];
  const int lane = threadIdx.x & 63, w = __builtin_amdgcn_readfirstlane(threadIdx.x >> 6);
  const int bid = blockIdx.x;
  const int s = bid & 1, kvh = (bid >> 1) & 15, b = bid >> 5;
  const int g = lane >> 4, c = lane & 15, h = (lane >> 3) & 1;
  const size_t base = ((size_t)(b * HKV + kvh) * SK + (SK - W) + s * 1024 + w * 128) * DH;
  const char* kp = (const char*)(kc + base) + c * 256 + g * 16;
  const char* vp = (const char*)(vc + base) + g * 1024 + c * 16;
  v4u qf[4];
#pragma unroll
  for (int t = 0; t < 4; ++t) qf[t] = *(const v4u*)(q + b * (HQ * DH) + (kvh * 2 + h) * DH + 32 * t + 8 * g);
  v4u kr[8][4], vr[8][4];
#pragma unroll
  for (int i = 0; i < 8; ++i)
#pragma unroll
    for (int t = 0; t < 4; ++t) kr[i][t] = __builtin_nontemporal_load((const v4u*)(kp + i * 4096 + t * 64));
#pragma unroll
  for (int i = 0; i < 3; ++i)
#pragma unroll
    for (int u = 0; u < 4; ++u) vr[i][u] = __builtin_nontemporal_load((const v4u*)(vp + i * 4096 + u * 256));
  __builtin_amdgcn_sched_barrier(0);
  f32x4 sc[8];
#pragma unroll
  for (int i = 0; i < 8; ++i) {
    f32x4 a = {0.f, 0.f, 0.f, 0.f};
#pragma unroll
    for (int t = 0; t < 4; ++t) a = mfma(kr[i][t], qf[t], a);
    sc[i] = a;
    if (i + 3 < 8) {
#pragma unroll
      for (int u = 0; u < 4; ++u) vr[i + 3][u] = __builtin_nontemporal_load((const v4u*)(vp + (i + 3) * 4096 + u * 256));
    }
    __builtin_amdgcn_sched_barrier(0);
  }
  const float scale = 0.08838834764831845f;
  float m = -INFINITY;
#pragma unroll
  for (int i = 0; i < 8; ++i)
#pragma unroll
    for (int r = 0; r < 4; ++r) {
      float v = rbf(rbf(sc[i][r]) * scale);
      sc[i][r] = v; m = fmaxf(m, v);
    }
  m = fmaxf(m, __shfl_xor(m, 16));
  m = fmaxf(m, __shfl_xor(m, 32));
  const float L2E = 1.4426950408889634f;
  float l = 0.f;
  unsigned msk[4];
#pragma unroll
  for (int d = 0; d < 4; ++d) msk[d] = (((lane & 7) >> 1) == d) ? ((lane & 1) ? 0xFFFF0000u : 0x0000FFFFu) : 0u;
  f32x4 o = {0.f, 0.f, 0.f, 0.f};
#pragma unroll
  for (int i = 0; i < 8; ++i) {
#pragma unroll
    for (int u = 0; u < 4; ++u) {
      float p = __builtin_amdgcn_exp2f((sc[i][u] - m) * L2E);
      l += p;
      unsigned pb = f2bf(p); pb |= pb << 16;
      v4u bp = {pb & msk[0], pb & msk[1], pb & msk[2], pb & msk[3]};
      o = mfma(vr[i][u], bp, o);
    }
  }
  l += __shfl_xor(l, 16);
  l += __shfl_xor(l, 32);
#pragma unroll
  for (int r = 0; r < 4; ++r) sm_o[w][h][8 * (4 * g + r) + (lane & 7)] = o[r];
  if (lane == 0 || lane == 8) { sm_ml[w][h][0] = m; sm_ml[w][h][1] = l; }
  __syncthreads();
  if (threadIdx.x < 256) {
    const int hh = threadIdx.x >> 7, d = threadIdx.x & 127;
    float M = -INFINITY;
#pragma unroll
    for (int i = 0; i < 8; ++i) M = fmaxf(M, sm_ml[i][hh][0]);
    float L = 0.f, acc = 0.f;
#pragma unroll
    for (int i = 0; i < 8; ++i) { float wt = __builtin_amdgcn_exp2f((sm_ml[i][hh][0] - M) * L2E); L += wt * sm_ml[i][hh][1]; acc += wt * sm_o[i][hh][d]; }
    float* pp = part + (size_t)((b * HQ + kvh * 2 + hh) * 2 + s) * 132;
    pp[4 + d] = acc;
    if (d == 0) { pp[0] = M; pp[1] = L; }
  }
}

__global__ void __launch_bounds__(512) k_oproj(const float* part, const u16* wo, const u16* x, u16* hout) {
  __shared__ u16 o[B][HQ * DH];
  __shared__ float red[64][B][8];
  const float L2E = 1.4426950408889634f;
  for (int i = threadIdx.x; i < B * HQ * DH; i += 512) {
    const int bh = i >> 7, d = i & 127;
    const float* p0 = part + (size_t)(bh * 2) * 132; const float* p1 = p0 + 132;
    const float M = fmaxf(p0[0], p1[0]);
    const float w0 = __builtin_amdgcn_exp2f((p0[0] - M) * L2E), w1 = __builtin_amdgcn_exp2f((p1[0] - M) * L2E);
    const float L = w0 * p0[1] + w1 * p1[1];
    (&o[0][0])[i] = f2bf((w0 * p0[4 + d] + w1 * p1[4 + d]) / L);
  }
  __syncthreads();
  const int cc = threadIdx.x & 7, kg = threadIdx.x >> 3;
  const int col = blockIdx.x * 8 + cc;
  float acc[B];
#pragma unroll
  for (int b = 0; b < B; ++b) acc[b] = 0.f;
  for (int k = kg; k < HQ * DH; k += 64) {
    float wv = bf2f(wo[(size_t)k * D + col]);
#pragma unroll
    for (int b = 0; b < B; ++b) acc[b] += bf2f(o[b][k]) * wv;
  }
#pragma unroll
  for (int b = 0; b < B; ++b) red[kg][b][cc] = acc[b];
  __syncthreads();
  if (threadIdx.x < 64) {
    const int b = threadIdx.x >> 3;
    float s = 0.f;
    for (int i = 0; i < 64; ++i) s += red[i][b][cc];
    hout[b * D + col] = f2bf(bf2f(x[b * D + col]) + rbf(s));
  }
}

__global__ void __launch_bounds__(512) k_ffn1(const u16* hin, const u16* w1, u16* h1) {
  __shared__ float xn[B][D];
  __shared__ float red[8][B][64];
  const int lane = threadIdx.x & 63, w = threadIdx.x >> 6;
  ln_row(hin + w * D, xn[w], lane);
  __syncthreads();
  const int col = blockIdx.x * 64 + lane;
  float acc[B];
#pragma unroll
  for (int b = 0; b < B; ++b) acc[b] = 0.f;
  for (int k = w * 72; k < w * 72 + 72; ++k) {
    float wv = bf2f(w1[(size_t)k * DFF + col]);
#pragma unroll
    for (int b = 0; b < B; ++b) acc[b] += xn[b][k] * wv;
  }
#pragma unroll
  for (int b = 0; b < B; ++b) red[w][b][lane] = acc[b];
  __syncthreads();
  float s = 0.f;
#pragma unroll
  for (int i = 0; i < 8; ++i) s += red[i][w][lane];
  h1[w * DFF + col] = f2bf(fmaxf(rbf(s), 0.f));
}

__global__ void __launch_bounds__(512) k_ffn2(const u16* h1, const u16* w2, const u16* hin, u16* out) {
  __shared__ float red[64][B][8];
  const int cc = threadIdx.x & 7, kg = threadIdx.x >> 3;
  const int col = blockIdx.x * 8 + cc;
  float acc[B];
#pragma unroll
  for (int b = 0; b < B; ++b) acc[b] = 0.f;
  for (int k = kg; k < DFF; k += 64) {
    float wv = bf2f(w2[(size_t)k * D + col]);
#pragma unroll
    for (int b = 0; b < B; ++b) acc[b] += bf2f(h1[b * DFF + k]) * wv;
  }
#pragma unroll
  for (int b = 0; b < B; ++b) red[kg][b][cc] = acc[b];
  __syncthreads();
  if (threadIdx.x < 64) {
    const int b = threadIdx.x >> 3;
    float s = 0.f;
    for (int i = 0; i < 64; ++i) s += red[i][b][cc];
    out[b * D + col] = f2bf(bf2f(hin[b * D + col]) + rbf(s));
  }
}

extern "C" void kernel_launch(void* const* d_in, const int* in_sizes, int n_in,
                              void* d_out, int out_size, void* d_ws, size_t ws_size,
                              hipStream_t stream) {
  const u16* x = (const u16*)d_in[0];
  const u16* kc = (const u16*)d_in[1];
  const u16* vc = (const u16*)d_in[2];
  const u16* wq = (const u16*)d_in[3];
  const u16* wo = (const u16*)d_in[4];
  const u16* w1 = (const u16*)d_in[6];
  const u16* w2 = (const u16*)d_in[7];
  char* ws = (char*)d_ws;
  u16* q = (u16*)ws;
  float* part = (float*)(ws + 65536);
  u16* h = (u16*)(ws + 65536 + 524288);
  u16* h1 = (u16*)(ws + 65536 + 524288 + 65536);
  k_qproj<<<64, 512, 0, stream>>>(x, wq, q);
  k_attn<<<256, 512, 0, stream>>>(kc, vc, q, part);
  k_oproj<<<72, 512, 0, stream>>>(part, wo, x, h);
  k_ffn1<<<72, 512, 0, stream>>>(h, w1, h1);
  k_ffn2<<<72, 512, 0, stream>>>(h1, w2, h, (u16*)d_out);
}
```

```cpp
#define STAMPS 1
#define TSTREAM 1980
#define CODE_BYTES 25000
#include <hip/hip_runtime.h>
#include <stdint.h>

typedef unsigned v4u __attribute__((ext_vector_type(4)));
typedef unsigned v2u __attribute__((ext_vector_type(2)));
typedef float f32x4 __attribute__((ext_vector_type(4)));
typedef float f32x2 __attribute__((ext_vector_type(2)));
typedef short bf16x8 __attribute__((ext_vector_type(8)));
typedef short v4s __attribute__((ext_vector_type(4)));
typedef short v2s __attribute__((ext_vector_type(2)));
typedef __bf16 bf16x2 __attribute__((ext_vector_type(2)));
typedef unsigned long long u64;
typedef unsigned short u16;
typedef __amdgpu_buffer_rsrc_t rsrc_t;
#define LAS __attribute__((address_space(3)))
#define GAS __attribute__((address_space(1)))

#define NREP 8
#define GATE_O 1
#define GATE_F 1
#define WINT 6
#define ABL 0
#define BURST 14
#define OBLK 3
#define FBLK 0
#define T0OFF 0
#define QT 2
#define CAP 0
#define QFAST 16
#define QSTART 280
#define D0 40
#define CW 7
#define TMINQ 1300
#define QCOST 7
#define GATE_P 3
#define TAUW 19
#define CAPT 1
#define WARM 0
#define WPACE 0

constexpr int DM = 576, HD = 4096, SK = 8192, DH = 128, WND = 2048, DFF = 4608;
constexpr unsigned SENT = 0xFFFFFFFFu;
#define TAILST 16
#define TAILLD 16
constexpr int AUX_NT = 2, AUX_SC1 = TAILLD, AUX_SC1NT = 18;
constexpr unsigned SPIN_CAP = 1u << 17;

constexpr int WS_E = 0;
constexpr int WS_READY = 4096;
constexpr int WS_READY2 = 6144;
constexpr int WS_QP = 8192;
constexpr int WS_O = WS_QP + 3 * 8 * 4096 * 4;
constexpr int WS_LSE = WS_O + 32 * 2 * 2 * 8 * 128;
constexpr int WS_R = WS_LSE + 32 * 128;
constexpr int WS_H = WS_R + 9 * 8 * 8 * 256;
constexpr int WS_P = WS_H + 16 * 9216;
constexpr int WS_END = WS_P + 144 * 72 * 128;
constexpr int WS_ST = (WS_END + 4095) & ~4095;

constexpr int L_CTL = 0;
constexpr int L_Q = 64;
constexpr int L_ML = 640;
constexpr int L_H1 = 768;
constexpr int L_PART = 2048;
constexpr int L_BFR = L_PART + 8192;
constexpr int L_HROW = L_BFR + 9216;
constexpr int L_W = L_HROW + 9216;
constexpr int L_END = L_W + 14 * 8192;
static_assert(L_W % 128 == 0 && L_END <= 160 * 1024, "lds");

#define WSPOS 0
static inline size_t ws_off(size_t ws_size) { return WSPOS ? ((ws_size - (size_t)WSPOS * 1048576) & ~(size_t)0x1FFFFF) : 0; }
struct Args { const u16 *x, *kc, *vc, *wq, *wo, *w1, *w2; u16* out; unsigned char* ws; };

__device__ __forceinline__ rsrc_t mkrs(const void* p, int bytes) { return __builtin_amdgcn_make_buffer_rsrc((void*)p, 0, bytes, 0x00020000); }
__device__ __forceinline__ float bflo(unsigned u) { return __builtin_bit_cast(float, u << 16); }
__device__ __forceinline__ float bfhi(unsigned u) { return __builtin_bit_cast(float, u & 0xFFFF0000u); }
__device__ __forceinline__ f32x2 up2(unsigned u) { return f32x2{bflo(u), bfhi(u)}; }
__device__ __forceinline__ unsigned pk(float lo, float hi) {
  const bf16x2 v = {(__bf16)lo, (__bf16)hi};
  unsigned r = __builtin_bit_cast(unsigned, v);
  asm("" : "+v"(r));
  return r;
}
__device__ __forceinline__ unsigned pk2(f32x2 v) { return pk(v[0], v[1]); }
__device__ __forceinline__ float rbf(float f) { return bflo(pk(f, f)); }
__device__ __forceinline__ unsigned nosent(unsigned u) { return u < 0xFFFFFFFEu ? u : 0xFFFFFFFEu; }
__device__ __forceinline__ unsigned nosentf(float f) { return nosent(__builtin_bit_cast(unsigned, f)); }
__device__ __forceinline__ f32x4 mfma(v4u a, v4u b, f32x4 c) {
  return __builtin_amdgcn_mfma_f32_16x16x32_bf16(__builtin_bit_cast(bf16x8, a), __builtin_bit_cast(bf16x8, b), c, 0, 0, 0);
}
template <int CTRL> __device__ __forceinline__ float dpp(float x) {
  return __builtin_bit_cast(float, __builtin_amdgcn_mov_dpp(__builtin_bit_cast(int, x), CTRL, 0xf, 0xf, true));
}
constexpr int XOR1 = 0xB1, XOR2 = 0x4E, HMIR = 0x141, ROR8 = 0x128;
__device__ __forceinline__ float x16_sum(float x) {
  auto s = __builtin_amdgcn_permlane16_swap(__float_as_uint(x), __float_as_uint(x), false, false);
  return __uint_as_float(s[0]) + __uint_as_float(s[1]);
}
__device__ __forceinline__ float x32_sum(float x) {
  auto s = __builtin_amdgcn_permlane32_swap(__float_as_uint(x), __float_as_uint(x), false, false);
  return __uint_as_float(s[0]) + __uint_as_float(s[1]);
}
__device__ __forceinline__ float x16_max(float x) {
  auto s = __builtin_amdgcn_permlane16_swap(__float_as_uint(x), __float_as_uint(x), false, false);
  return fmaxf(__uint_as_float(s[0]), __uint_as_float(s[1]));
}
__device__ __forceinline__ float x32_max(float x) {
  auto s = __builtin_amdgcn_permlane32_swap(__float_as_uint(x), __float_as_uint(x), false, false);
  return fmaxf(__uint_as_float(s[0]), __uint_as_float(s[1]));
}
__device__ __forceinline__ float sum_b012(float x) { x += dpp<XOR1>(x); x += dpp<XOR2>(x); return x + dpp<HMIR>(x); }
__device__ __forceinline__ float sum_b345(float x) { x += dpp<ROR8>(x); return x32_sum(x16_sum(x)); }
__device__ __forceinline__ float ln_inv(float var) { return __builtin_amdgcn_rcpf(rbf(__builtin_amdgcn_sqrtf(rbf(rbf(var) + 1e-5f)))); }
__device__ __forceinline__ unsigned ln_norm(f32x2 v, float mb, float inv) { return pk2(up2(pk2(v - mb)) * inv); }
__device__ __forceinline__ unsigned umax(unsigned a, unsigned b) { return a > b ? a : b; }
__device__ __forceinline__ unsigned mx(v4u v) { return umax(umax(v.x, v.y), umax(v.z, v.w)); }
__device__ __forceinline__ unsigned mx(v2u v) { return umax(v.x, v.y); }
#define LD128(rs, vo, so, aux) __builtin_amdgcn_raw_buffer_load_b128(rs, vo, so, aux)
#define LD64(rs, vo, so, aux) __builtin_amdgcn_raw_buffer_load_b64(rs, vo, so, aux)
#define LD32(rs, vo, so, aux) __builtin_amdgcn_raw_buffer_load_b32(rs, vo, so, aux)
#define ST128(v, rs, vo, so) __builtin_amdgcn_raw_buffer_store_b128(v, rs, (vo) + (so), 0, TAILST)
#define ST64(v, rs, vo, so) __builtin_amdgcn_raw_buffer_store_b64(v, rs, vo, so, TAILST)
#define ST32(v, rs, vo, so) __builtin_amdgcn_raw_buffer_store_b32(v, rs, vo, so, TAILST)
#define SN128(v, rs, vo, so) __builtin_amdgcn_raw_buffer_store_b128(v, rs, (vo) + (so), 0, AUX_SC1NT)
#define SN64(v, rs, vo, so) __builtin_amdgcn_raw_buffer_store_b64(v, rs, vo, so, AUX_SC1NT)
#define SN32(v, rs, vo, so) __builtin_amdgcn_raw_buffer_store_b32(v, rs, vo, so, AUX_SC1NT)
#define CBAR() asm volatile("" ::: "memory")
#define PIN(x) asm volatile("" : "+v"(x))
#define SB() __builtin_amdgcn_sched_barrier(0)
#define DRAIN() asm volatile("s_waitcnt vmcnt(0)" ::: "memory")
#define LDSWAIT() asm volatile("s_waitcnt lgkmcnt(0)" ::: "memory")
#define SPIN _Pragma("nounroll") for (unsigned spins = 0; spins < SPIN_CAP; ++spins)
__device__ __forceinline__ void wg_barrier() { LDSWAIT(); __builtin_amdgcn_s_barrier(); CBAR(); }
#define STAMP(i) do { SB(); ts[i] = __builtin_amdgcn_s_memrealtime(); SB(); } while (0)
#define EXIT() do { if (lane == 0) { _Pragma("unroll") for (int i_ = 0; i_ < 16; ++i_) ((GAS u64*)(a.ws + WS_ST))[(blockIdx.x * 8 + wv) * 16 + i_] = ts[i_]; } return; } while (0)

__device__ __forceinline__ void lds_wait(LAS unsigned char* p, unsigned want) {
  unsigned f;
  asm volatile("1:\n\tds_read_b32 %0, %1\n\ts_waitcnt lgkmcnt(0)\n\tv_cmp_le_u32 vcc, %2, %0\n\ts_cbranch_vccnz 2f\n\ts_sleep 1\n\ts_branch 1b\n2:"
               : "=&v"(f) : "v"((unsigned)(size_t)p), "s"(want) : "vcc", "memory");
}
template <int CAPN> __device__ __forceinline__ void pace(unsigned ctr, unsigned vtau, unsigned t0) {
  unsigned v, s;
  asm volatile("s_waitcnt vmcnt(%6)\n\ts_memrealtime vcc\n\ts_mov_b64 exec, 1\n\ts_waitcnt lgkmcnt(0)\n\ts_sub_u32 %1, vcc_lo, %4\n\ts_lshl_b32 %1, %1, 4\n\tv_mov_b32 %0, %1\n\t"
               "ds_max_u32 %2, %0\n\tds_add_rtn_u32 %0, %2, %3\n\ts_mov_b64 exec, -1\n\ts_waitcnt lgkmcnt(0)\n\tv_readfirstlane_b32 %1, %0\n\ts_nop 0\n\t"
               "s_lshr_b32 %1, %1, 4\n\ts_add_u32 %1, %1, %4\n\ts_sub_u32 %1, %1, %5\n"
               "1:\n\ts_sub_u32 vcc_lo, vcc_lo, %1\n\ts_cmp_lt_i32 vcc_lo, 0\n\ts_cbranch_scc0 2f\n\ts_sleep 1\n\ts_memrealtime vcc\n\ts_waitcnt lgkmcnt(0)\n\ts_branch 1b\n2:"
               : "=&v"(v), "=&s"(s) : "v"(ctr), "v"(vtau), "s"(t0), "n"(BURST), "n"(CAPN) : "vcc", "scc", "memory");
}
__device__ __forceinline__ void wait_until(unsigned t) {
  asm volatile("1:\n\ts_memrealtime vcc\n\ts_waitcnt lgkmcnt(0)\n\ts_sub_u32 vcc_lo, vcc_lo, %0\n\ts_cmp_lt_i32 vcc_lo, 0\n\ts_cbranch_scc0 2f\n\ts_sleep 1\n\ts_branch 1b\n2:"
               :: "s"(t) : "vcc", "scc", "memory");
}
__device__ __forceinline__ void load_block(rsrc_t rs, int soff, int pitch, LAS unsigned char* dst, int lane) {
  const int rowoff = (lane >> 3) * pitch, c0 = (lane & 7) ^ ((lane >> 4) & 1);
#pragma unroll
  for (int p = 0; p < 8; ++p) {
    const int cp = ((p & 1) << 1) | (((p >> 2) & 1) << 2);
    if (WPACE && (p & 1) == 0) asm volatile("s_waitcnt vmcnt(%0)" :: "n"(WPACE > 2 ? WPACE - 2 : 0) : "memory");
    __builtin_amdgcn_raw_ptr_buffer_load_lds(rs, (LAS void*)(dst + p * 1024), 16, rowoff + ((c0 ^ cp) << 4), soff + p * 8 * pitch, 0, AUX_NT);
  }
}
__device__ __forceinline__ v4u tr_frag(LAS unsigned char* p) {
  const v4s lo = __builtin_amdgcn_ds_read_tr16_b64_v4i16((LAS v4s*)p);
  const v4s hi = __builtin_amdgcn_ds_read_tr16_b64_v4i16((LAS v4s*)(p + 512));
  const v2u l = __builtin_bit_cast(v2u, lo), h = __builtin_bit_cast(v2u, hi);
  return v4u{l.x, l.y, h.x, h.y};
}
__device__ __forceinline__ f32x4 fold_half4(f32x4 x) {
  f32x4 t;
#pragma unroll
  for (int i = 0; i < 4; ++i) { auto s = __builtin_amdgcn_permlane32_swap(__float_as_uint(x[i]), __float_as_uint(x[i]), false, false); x[i] = __uint_as_float(s[0]); t[i] = __uint_as_float(s[1]); }
#pragma unroll
  for (int i = 0; i < 4; ++i) x[i] += dpp<ROR8>(t[i]);
  return x;
}

__device__ __forceinline__ void ln_row(v4u hv, unsigned he, float emask, unsigned a_hn, unsigned a_he) {
  f32x2 v[5];
#pragma unroll
  for (int d = 0; d < 4; ++d) v[d] = up2(hv[d]);
  v[4] = up2(he);
  const float piv = __builtin_bit_cast(float, __builtin_amdgcn_readfirstlane(__builtin_bit_cast(int, v[0][0])));
  f32x2 d[5];
#pragma unroll
  for (int i = 0; i < 5; ++i) d[i] = v[i] - piv;
  d[4] *= emask;
  const f32x2 t1 = ((d[0] + d[1]) + (d[2] + d[3])) + d[4];
  const f32x2 t2 = (d[0] * d[0] + d[1] * d[1]) + (d[2] * d[2] + (d[3] * d[3] + d[4] * d[4]));
  float s1 = t1[0] + t1[1], s2 = t2[0] + t2[1];
  s1 += dpp<XOR1>(s1); s2 += dpp<XOR1>(s2);
  s1 += dpp<XOR2>(s1); s2 += dpp<XOR2>(s2);
  s1 += dpp<HMIR>(s1); s2 += dpp<HMIR>(s2);
  s1 += dpp<ROR8>(s1); s2 += dpp<ROR8>(s2);
  auto sw = __builtin_amdgcn_permlane16_swap(__float_as_uint(s1), __float_as_uint(s2), false, false);
  float c = __uint_as_float(sw[0]) + __uint_as_float(sw[1]);
  c = x32_sum(c);
  s1 = __builtin_bit_cast(float, __builtin_amdgcn_readlane(__builtin_bit_cast(int, c), 0)) * (1.0f / DM);
  s2 = __builtin_bit_cast(float, __builtin_amdgcn_readlane(__builtin_bit_cast(int, c), 16)) * (1.0f / DM);
  const float inv = ln_inv(fmaxf(fmaf(-s1, s1, s2), 0.f)), mb = rbf(piv + s1);
  *(LAS v4u*)(size_t)a_hn = v4u{ln_norm(v[0], mb, inv), ln_norm(v[1], mb, inv), ln_norm(v[2], mb, inv), ln_norm(v[3], mb, inv)};
  *(LAS unsigned*)(size_t)a_he = ln_norm(v[4], mb, inv);
}

__global__ void __launch_bounds__(512, 2) fused_decode_block(Args a) {
  extern __shared__ __attribute__((aligned(128))) unsigned char smem[];
  LAS unsigned char* L = (LAS unsigned char*)smem;
  const int lane = threadIdx.x & 63, wv = __builtin_amdgcn_readfirstlane(threadIdx.x >> 6);
  const int bid = blockIdx.x, xg = bid & 7, r = bid >> 3;
  u64 ts[16];
#pragma unroll
  for (int i = 0; i < 16; ++i) ts[i] = 0;
  STAMP(0);
  unsigned warm[4] = {0, 0, 0, 0};
  {
    u64 pc;
    asm volatile("s_getpc_b64 %0" : "=s"(pc));
    warm[0] = LD32(mkrs((const void*)pc, CODE_BYTES), CODE_BYTES > 32768 ? (lane & 1) * 32768 : 0, (wv * 32 + r) * 128, 0);
  }
  constexpr unsigned tau_s = (unsigned)((TSTREAM - D0) * 16) / (127u + QCOST);
  const unsigned t_my = (unsigned)__builtin_amdgcn_s_memrealtime();
  if (threadIdx.x < 12) ((LAS unsigned*)(L + L_CTL))[lane] = lane == 8 ? t_my : (lane == 7 ? D0 * 16 + 8 * tau_s : 0u);
  SB();
  const int kvh = 2 * xg + (r & 1), sp = (r >> 1) & 1, bb = r >> 2;
  const int n16 = lane & 15, g4 = lane >> 4;
  const unsigned hrot = (bid * 2654435761u) >> 20;
  const int wrot = (wv + (hrot >> 3)) & 7, trot = (hrot & 7) << 15;
  const size_t kvbase = ((size_t)(bb * 16 + kvh) * SK + (SK - WND) + sp * 1024 + wrot * 16) * DH;
  const rsrc_t rk = mkrs(a.kc + kvbase, 1024 * 256 - wrot * 4096);
  const int ko = (n16 >> 2) * 256 + g4 * 64 + (lane & 3) * 16, vo = lane * 16;
  v4u kr[WINT][4], vr[WINT][4];
  wg_barrier();
  wait_until(t_my + D0 - BURST + ((wv * tau_s) >> 4));
  SB();
#pragma unroll
  for (int t = 0; t < 4; ++t) kr[0][t] = LD128(rk, ko + t * 1024, trot, AUX_NT);
  SB();
  if (r >= 27) STAMP(9);
  const rsrc_t rv = mkrs(a.vc + kvbase, 1024 * 256 - wrot * 4096);
  const bool role_o = r < 9, role_f = r >= 9 && r < 27, role_q = r >= 8;
  const int fe = r - 9, ff = 9 * xg + (fe >> 1), fh = fe & 1;
  const int fr_w = (lane ^ ((lane >> 3) & 2)) * 16;
  const int fr_r = ((n16 & 7) * 8 + (((n16 >> 3) * 4 + g4) ^ (n16 & 2))) * 16;
  const rsrc_t ws = mkrs(a.ws, WS_END);
  const float L2E = 1.4426950408889634f;
  unsigned tau = tau_s;
  unsigned pctr = (unsigned)(size_t)(L + L_CTL + 28);
  const unsigned t_start = __builtin_amdgcn_readfirstlane(*(volatile LAS unsigned*)(L + L_CTL + 32));
  u64 tag = 0;

  auto clears = [&]() {
    const v4u S4 = {SENT, SENT, SENT, SENT};
    if (!(ABL & 1)) {
    SN64((v2u{SENT, SENT}), ws, WS_O + ((2 * kvh * 2 + g4) * 2 + sp) * 1024 + bb * 128 + n16 * 8, 0);
    if (lane < 2) __builtin_amdgcn_raw_buffer_store_b32(SENT, ws, WS_LSE + (2 * kvh + lane) * 128 + bb * 8 + sp * 4, 0, 16);
    if (role_o) {
      const int o = WS_R + ((r * 8 + g4) * 8 + xg) * 256 + n16 * 16;
      SN128(S4, ws, o, 0); SN128(S4, ws, o, 4 * 8 * 256);
#pragma unroll
      for (int i = 0; i < (NREP + 3) / 4; ++i) if (g4 + 4 * i < NREP) SN64((v2u{SENT, SENT}), ws, WS_H + (g4 + 4 * i) * 9216 + xg * 1152 + r * 128 + n16 * 8, 0);
    }
    }
    SB();
  };
  if (__builtin_expect(wv == 0, 0)) {
    __builtin_amdgcn_s_setprio(3);
    { unsigned tq = QCOST * tau_s; SB(); pace<63>(pctr, tq, t_start); SB(); }
    if (r >= 27) STAMP(10);
    if (role_q) {
      const int qp_ = xg * 24 + r - 8, qj = qp_ & 63, qs = qp_ >> 6;
      v4u xr[9], wr[3][8];
      const rsrc_t rq = mkrs(a.wq, (ABL & 8) ? 0 : DM * HD * 2);
      const int wo_ = (32 * (n16 >> 3) + 8 * g4) * (HD * 2) + (n16 & 7) * 16;
      const rsrc_t rx = mkrs(a.x, 8 * DM * 2);
#pragma unroll
      for (int i = 0; i < 9; ++i) { int kb = 3 * qs + i; kb = kb >= 9 ? kb - 9 : kb; xr[i] = LD128(rx, (lane >> 3) * (DM * 2) + (lane & 7) * 16, kb * 128, 0); }
#pragma unroll
      for (int i = 0; i < 3; ++i)
#pragma unroll
        for (int t = 0; t < 8; ++t) wr[i][t] = LD128(rq, wo_, qj * 128 + (192 * qs + 64 * i + t) * (HD * 2), AUX_NT);
      SB();
      f32x2 s = {0.f, 0.f}, s_ = {0.f, 0.f};
#pragma unroll
      for (int i = 0; i < 9; ++i) { s += up2(xr[i][0]); s_ += up2(xr[i][1]); s += up2(xr[i][2]); s_ += up2(xr[i][3]); }
      s += s_;
      const float mean = sum_b012(s[0] + s[1]) * (1.0f / DM);
      f32x2 q = {0.f, 0.f}, q_ = {0.f, 0.f};
#pragma unroll
      for (int i = 0; i < 9; ++i)
#pragma unroll
        for (int d = 0; d < 4; d += 2) { const f32x2 u = up2(xr[i][d]) - mean, v = up2(xr[i][d + 1]) - mean; q += u * u; q_ += v * v; }
      q += q_;
      const float inv = ln_inv(sum_b012(q[0] + q[1]) * (1.0f / DM)), mb = rbf(mean);
#pragma unroll
      for (int i = 0; i < 3; ++i)
        *(LAS v4u*)(L + L_BFR + i * 1024 + fr_w) = v4u{ln_norm(up2(xr[i][0]), mb, inv), ln_norm(up2(xr[i][1]), mb, inv), ln_norm(up2(xr[i][2]), mb, inv), ln_norm(up2(xr[i][3]), mb, inv)};
      LDSWAIT();
      STAMP(2);
      f32x4 acc[8];
#pragma unroll
      for (int i = 0; i < 3; ++i) {
        const v4u bf = *(const LAS v4u*)(L + L_BFR + i * 1024 + fr_r);
#pragma unroll
        for (int j = 0; j < 8; ++j) {
          v4u af;
#pragma unroll
          for (int d = 0; d < 4; ++d) af[d] = __builtin_amdgcn_perm(wr[i][2 * d + 1][j >> 1], wr[i][2 * d][j >> 1], (j & 1) ? 0x07060302u : 0x05040100u);
          acc[j] = mfma(af, bf, i ? acc[j] : f32x4{0.f, 0.f, 0.f, 0.f});
        }
      }
      if ((g4 >> 1) == (n16 >> 3)) {
        LAS f32x4* red = (LAS f32x4*)(L + L_HROW + (((g4 >> 1) * 8 + (n16 & 7)) * 68 + 32 * (g4 & 1)) * 4);
#pragma unroll
        for (int rr = 0; rr < 4; ++rr) {
          red[2 * rr] = f32x4{acc[0][rr], acc[1][rr], acc[2][rr], acc[3][rr]};
          red[2 * rr + 1] = f32x4{acc[4][rr], acc[5][rr], acc[6][rr], acc[7][rr]};
        }
      }
      LDSWAIT();
      STAMP(14);
      const int qo = WS_QP + (((g4 * 16 + (qj >> 2)) * 3 + qs) * 256 + 64 * (qj & 3) + 4 * n16) * 4;
#pragma unroll
      for (int i = 0; i < 2; ++i) {
        const LAS f32x4* red = (const LAS f32x4*)(L + L_HROW) + (4 * i + g4) * 17 + n16;
        SN128(__builtin_bit_cast(v4u, red[0] + red[8 * 17]), ws, (ABL & 2) ? WS_END : qo, i * 4 * 16 * 3 * 256 * 4);
      }
      STAMP(3);
    } else {
      const rsrc_t rw = mkrs(a.wo, HD * DM * 2);
#pragma unroll
      for (int kb = 0; kb < OBLK; ++kb) load_block(rw, (512 * xg + 64 * kb) * (DM * 2) + r * 128, DM * 2, L + L_W + kb * 8192, lane);
    }
    __builtin_amdgcn_s_waitcnt(0x0F70);
    DRAIN();
    lds_wait(L + L_CTL + 12, 1u);
    tag = *(volatile LAS u64*)(L + L_CTL + 16);
    if (lane == 0) {
      __builtin_amdgcn_raw_buffer_store_b64(__builtin_bit_cast(v2u, tag), ws, WS_READY + (role_q ? xg * 24 + r - 8 : 192 + xg * 8 + r) * 8, 0, 16);
    }
    STAMP(1);
  } else if (wv == 1) {
    tag = __builtin_bit_cast(u64, LD64(ws, WS_E, 0, 16));
    SB();
    clears();
    DRAIN();
    tag += 1;
    if (lane == 0) { *(volatile LAS u64*)(L + L_CTL + 16) = tag; *(volatile LAS unsigned*)(L + L_CTL + 12) = 1u; }
    STAMP(1);
  } else if (wv == 6) {
  }
  __builtin_amdgcn_s_setprio(2);

  float m_run, l_run;
  f32x4 o_acc = {0.f, 0.f, 0.f, 0.f};
  {
#define ISSUE(T) do { \
    if ((T) != 0) { SB(); pace<((T) <= CAPT ? CAP : 63)>(pctr, tau, t_start); SB(); \
    _Pragma("unroll") for (int t = 0; t < 4; ++t) kr[(T) % WINT][t] = LD128(rk, ko + t * 1024, (((T) << 15) + trot) & 0x38000, AUX_NT); } \
    SB(); pace<((T) <= CAPT ? CAP : 63)>(pctr, tau, t_start); SB(); \
    _Pragma("unroll") for (int t = 0; t < 4; ++t) vr[(T) % WINT][t] = LD128(rv, vo + t * 1024, (((T) << 15) + trot) & 0x38000, AUX_NT); } while (0)
    auto gather_q = [&]() {
      SB();
      __builtin_amdgcn_s_setprio(3);
      lds_wait(L + L_CTL + 12, 1u);
      const v2u tg = *(const LAS v2u*)(L + L_CTL + 16);
      const int li = lane < 12 ? lane : 0;
      SPIN {
        const v2u r0 = LD64(ws, WS_READY + ((li >> 2) * 64 + 4 * kvh + (li & 3)) * 8, 0, 16);
        if (__all(((r0.x ^ tg.x) | (r0.y ^ tg.y)) == 0u)) break;
        __builtin_amdgcn_s_sleep(20);
        CBAR();
      }
      CBAR();
      STAMP(15);
      const int go = WS_QP + (bb * 16 + kvh) * 3072 + lane * 16;
      const f32x4 q0 = __builtin_bit_cast(f32x4, LD128(ws, go, 0, AUX_SC1NT)), q1 = __builtin_bit_cast(f32x4, LD128(ws, go, 1024, AUX_SC1NT)), q2 = __builtin_bit_cast(f32x4, LD128(ws, go, 2048, AUX_SC1NT));
      const f32x4 qq = (q0 + q1) + q2;
      *(LAS v2u*)(L + L_Q + lane * 8) = v2u{pk(qq[0], qq[1]), pk(qq[2], qq[3])};
      LDSWAIT();
      if (lane == 0) *(volatile LAS unsigned*)(L + L_CTL) = 1u;
      __builtin_amdgcn_s_setprio(2);
      STAMP(4);
      SB();
    };
    ISSUE(0);
    if (r >= 27) STAMP(11);
    ISSUE(1);
    if (QT == 1 && wv == 2) gather_q();
    ISSUE(2);
    if (QT == 2 && wv == 2) gather_q();
    ISSUE(3); ISSUE(4); ISSUE(5);
    SB();
    lds_wait(L + L_CTL, 1u);
    SB();
    STAMP(5);
    asm volatile("" :: "v"(warm[0]), "v"(warm[1]), "v"(warm[2]), "v"(warm[3]));
    const v4u qf = *(const LAS v4u*)(L + L_Q + (n16 >> 3) * 256 + g4 * 64 + (lane & 3) * 16);
    unsigned msk[4];
#pragma unroll
    for (int d = 0; d < 4; ++d) msk[d] = (((lane & 7) >> 1) == d) ? ((lane & 1) ? 0xFFFF0000u : 0x0000FFFFu) : 0u;
    const float scale = 0.08838834764831845f;
#pragma unroll
    for (int T = 0; T < 8; ++T) {
      f32x4 s, dd[4];
#pragma unroll
      for (int t = 0; t < 4; ++t) dd[t] = mfma(kr[T % WINT][t], qf, f32x4{0.f, 0.f, 0.f, 0.f});
#pragma unroll
      for (int t = 0; t < 4; ++t) s[t] = (dpp<0x00>(dd[t][0]) + dpp<0x55>(dd[t][1])) + (dpp<0xAA>(dd[t][2]) + dpp<0xFF>(dd[t][3]));
      const unsigned d0 = pk2(up2(pk(s[0], s[1])) * scale), d1 = pk2(up2(pk(s[2], s[3])) * scale);
      float sv[4] = {bflo(d0), bfhi(d0), bflo(d1), bfhi(d1)};
      float mt = x32_max(x16_max(fmaxf(fmaxf(sv[0], sv[1]), fmaxf(sv[2], sv[3]))));
      if (T == 0) { m_run = mt; l_run = 0.f; }
      else {
        mt = fmaxf(mt, m_run);
        const float al = __builtin_amdgcn_exp2f((m_run - mt) * L2E);
        m_run = mt; l_run *= al; o_acc *= al;
      }
#pragma unroll
      for (int u = 0; u < 4; ++u) {
        const float p = __builtin_amdgcn_exp2f((sv[u] - m_run) * L2E);
        l_run += p;
        const unsigned pb = pk(p, p);
        o_acc = mfma(vr[T % WINT][u], v4u{pb & msk[0], pb & msk[1], pb & msk[2], pb & msk[3]}, o_acc);
      }
      SB();
      static_assert(WINT == 6, "issue order below");
      if (T == 2) ISSUE(6);
      if (T == 5) ISSUE(7);
      if (T == 5 && lane == 0) __hip_atomic_fetch_add((LAS unsigned*)(L + L_CTL + 8), 1u, __ATOMIC_RELAXED, __HIP_MEMORY_SCOPE_WORKGROUP);
      SB();
    }
#undef ISSUE
  }
  l_run = x32_sum(x16_sum(l_run));
#pragma unroll
  for (int rr = 0; rr < 4; ++rr) ((LAS float*)(L + L_PART))[wv * 256 + (n16 >> 3) * 128 + 32 * g4 + 8 * rr + (lane & 7)] = o_acc[rr];
  if ((lane & 55) == 0) *(LAS f32x2*)(L + L_ML + (wv * 2 + (lane >> 3)) * 8) = f32x2{m_run, l_run};
  CBAR();
  unsigned arr = 0;
  if (lane == 0) arr = __hip_atomic_fetch_add((LAS unsigned*)(L + L_CTL + 4), 1u, __ATOMIC_RELAXED, __HIP_MEMORY_SCOPE_WORKGROUP);
  arr = __builtin_amdgcn_readfirstlane(arr);
  CBAR();
  STAMP(6);
  if (arr == 7) {
    const int h = lane >> 5;
    f32x2 ml[8]; f32x4 ov[8];
#pragma unroll
    for (int w = 0; w < 8; ++w) ml[w] = *(const LAS f32x2*)(L + L_ML + (w * 2 + h) * 8);
#pragma unroll
    for (int w = 0; w < 8; ++w) ov[w] = *(const LAS f32x4*)(L + L_PART + w * 1024 + lane * 16);
    SB();
    float M = ml[0][0];
#pragma unroll
    for (int w = 1; w < 8; ++w) M = fmaxf(M, ml[w][0]);
    float Ls = 0.f; f32x4 o = {0.f, 0.f, 0.f, 0.f};
#pragma unroll
    for (int w = 0; w < 8; ++w) { const float e = __builtin_amdgcn_exp2f((ml[w][0] - M) * L2E); Ls = fmaf(e, ml[w][1], Ls); o += e * ov[w]; }
    o *= __builtin_amdgcn_rcpf(Ls);
    ST64((v2u{nosent(pk(o[0], o[1])), nosent(pk(o[2], o[3]))}), ws, WS_O + ((4 * kvh + g4) * 2 + sp) * 1024 + bb * 128 + n16 * 8, 0);
    if ((lane & 31) == 0) ST32(nosentf(fmaf(M, L2E, __builtin_amdgcn_logf(Ls))), ws, WS_LSE + (2 * kvh + h) * 128 + bb * 8 + sp * 4, 0);
    CBAR();
    if (lane == 0) *(volatile LAS unsigned*)(L + L_CTL + 24) = 1u;
    STAMP(7);
  }
  if ((!role_o && !role_f) || (ABL & 4)) EXIT();

  if (arr < 4) {
    __builtin_amdgcn_s_setprio(0);
    unsigned tauw = TAUW * 16;
#define LATE(...) do { if (TAUW) pace<63>(pctr, tauw, t_start); load_block(__VA_ARGS__); } while (0)
    if (role_o) { if (GATE_O == 1) lds_wait(L + L_CTL + 8, 8u); if (GATE_O == 2) lds_wait(L + L_CTL + 4, 8u); if (GATE_O == 3) lds_wait(L + L_CTL + 24, 1u); }
    else { if (GATE_F == 1) lds_wait(L + L_CTL + 8, 8u); if (GATE_F == 2) lds_wait(L + L_CTL + 4, 8u); if (GATE_F == 3) lds_wait(L + L_CTL + 24, 1u); }
    STAMP(13);
    if (role_o) {
      const rsrc_t rw = mkrs(a.wo, HD * DM * 2);
#pragma unroll
      for (int i = 0; i < 2; ++i) { const int kb = 7 - arr - 4 * i; if (kb >= (role_q ? 0 : OBLK)) LATE(rw, (512 * xg + 64 * kb) * (DM * 2) + r * 128, DM * 2, L + L_W + kb * 8192, lane); }
    } else {
      const rsrc_t r1 = mkrs(a.w1, DM * DFF * 2), r2 = mkrs(a.w2, DFF * DM * 2);
#pragma unroll
      for (int i = 0; i < 3; ++i) { const int kb = 8 - arr - 4 * i; if (kb >= 0) LATE(r1, 64 * kb * (DFF * 2) + ff * 128, DFF * 2, L + L_W + kb * 8192, lane); }
#pragma unroll
      for (int i = 0; i < 2; ++i) { const int cb = (3 - arr) + 4 * i; if (cb < 5 - fh) LATE(r2, 64 * ff * (DM * 2) + (5 * fh + cb) * 128, DM * 2, L + L_W + (9 + cb) * 8192, lane); }
    }
    DRAIN();
  } else if (arr == 5) {
    const v2u tg = *(const LAS v2u*)(L + L_CTL + 16);
    SPIN {
      const v4u r0 = LD128(ws, WS_READY + lane * 16, 0, 16), r1 = LD128(ws, WS_READY + lane * 16, 1024, 16);
      const unsigned bad = ((r0.x ^ tg.x) | (r0.z ^ tg.x) | (r1.x ^ tg.x) | (r1.z ^ tg.x)) | ((r0.y ^ tg.y) | (r0.w ^ tg.y) | (r1.y ^ tg.y) | (r1.w ^ tg.y));
      if (__all(bad == 0u)) break;
      __builtin_amdgcn_s_sleep(8);
      CBAR();
    }
    CBAR();
    if (bid == 0 && lane == 0) __builtin_amdgcn_raw_buffer_store_b64(tg, ws, WS_E, 0, 16);
  } else if (arr == 4 && role_f) {
    if (GATE_P == 1) lds_wait(L + L_CTL + 8, 8u);
    if (GATE_P == 3) lds_wait(L + L_CTL + 24, 1u);
    const v4u S4 = {SENT, SENT, SENT, SENT};
#pragma unroll
    for (int i = 0; i < 9; ++i) ST128(S4, ws, WS_P + (xg * 18 + fe) * 9216 + lane * 16, i * 1024);
    DRAIN();
    if (lane == 0) __builtin_amdgcn_raw_buffer_store_b64(*(const LAS v2u*)(L + L_CTL + 16), ws, WS_READY2 + (xg * 18 + fe) * 8, 0, 16);
  }
  wg_barrier();
  STAMP(8);
  const int tq = (lane >> 2) & 3, tp = lane & 3;
  const int trick_off = (32 * (tp >> 1) + 8 * g4 + tq) * 128 + ((wv ^ ((tq >> 1) | ((g4 & 1) << 1) | ((tp >> 1) << 2))) << 4) + (tp & 1) * 8;

  if (role_o) {
    v4u wf[8];
#pragma unroll
    for (int kb = 0; kb < 8; ++kb) wf[kb] = tr_frag(L + L_W + kb * 8192 + trick_off);
    v2u xres = LD64(mkrs(a.x, 8 * DM * 2), (xg * DM + 64 * r + 4 * n16) * 2, 0, 0);
    const int oo = WS_O + (8 * xg + wv) * 2048 + lane * 16;
    const int lo = WS_LSE + (4 * xg + (wv >> 1)) * 128 + (lane >> 3) * 8;
    int so = (lane < 32 && n16 < 8) ? WS_R + ((r * 8 + n16) * 8 + xg) * 256 + (8 * wv + 4 * g4) * 4 : WS_END;
    int ro = WS_R + (r * 8 + xg) * 2048 + lane * 16;
    int ho = WS_H + g4 * 9216 + xg * 1152 + r * 128 + n16 * 8;
    unsigned lbw = (unsigned)(size_t)(L + L_BFR + wv * 1024 + fr_w), lb = (unsigned)(size_t)(L + L_BFR + fr_r);
    PIN(so); PIN(ro); PIN(ho); PIN(lb); PIN(lbw); PIN(xres);
#pragma unroll
    for (int kb = 0; kb < 8; ++kb) PIN(wf[kb]);
    v4u o0, o1; v2u ls;
    SPIN {
      o0 = LD128(ws, oo, 0, AUX_SC1); o1 = LD128(ws, oo, 1024, AUX_SC1); ls = LD64(ws, lo, 0, AUX_SC1);
      if (__all(umax(umax(mx(o0), mx(o1)), mx(ls)) != SENT)) break;
      CBAR();
    }
    STAMP(9);
    {
      const float dl = __uint_as_float(ls.y) - __uint_as_float(ls.x);
      const float w0 = __builtin_amdgcn_rcpf(1.0f + __builtin_amdgcn_exp2f(dl)), w1 = __builtin_amdgcn_rcpf(1.0f + __builtin_amdgcn_exp2f(-dl));
      v4u bf;
#pragma unroll
      for (int d = 0; d < 4; ++d) bf[d] = pk2(up2(o0[d]) * w0 + up2(o1[d]) * w1);
      *(LAS v4u*)(size_t)lbw = bf;
    }
    wg_barrier();
    v4u bfr[8];
#pragma unroll
    for (int kb = 0; kb < 8; ++kb) bfr[kb] = *(const LAS v4u*)(size_t)(lb + kb * 1024);
    SB();
    f32x4 acc = {0.f, 0.f, 0.f, 0.f};
#pragma unroll
    for (int kb = 0; kb < 8; ++kb) acc = mfma(wf[kb], bfr[kb], acc);
    acc = fold_half4(acc);
    ST128((v4u{nosentf(acc[0]), nosentf(acc[1]), nosentf(acc[2]), nosentf(acc[3])}), ws, so, 0);
    STAMP(10);
    if (wv != 0) EXIT();
    v4u p0, p1;
    SPIN {
      p0 = LD128(ws, ro, 0, AUX_SC1); p1 = LD128(ws, ro, 1024, AUX_SC1);
      if (__all(umax(mx(p0), mx(p1)) != SENT)) break;
      CBAR();
    }
    STAMP(11);
    f32x4 sm = __builtin_bit_cast(f32x4, p0) + __builtin_bit_cast(f32x4, p1);
#pragma unroll
    for (int d = 0; d < 4; ++d) sm[d] = x16_sum(sm[d]);
#pragma unroll
    for (int d = 0; d < 4; ++d) sm[d] = x32_sum(sm[d]);
    const v2u hv = {nosent(pk2(up2(xres.x) + up2(pk(sm[0], sm[1])))), nosent(pk2(up2(xres.y) + up2(pk(sm[2], sm[3]))))};
#pragma unroll
    for (int i = 0; i < (NREP + 3) / 4; ++i) if (g4 + 4 * i < NREP) ST64(hv, ws, ho, 4 * i * 9216);
    STAMP(12);
    EXIT();
  }

  v4u w1f[9], w2f[2][2][2];
#pragma unroll
  for (int kb = 0; kb < 9; ++kb) w1f[kb] = tr_frag(L + L_W + kb * 8192 + trick_off);
  const bool second = fh == 0 && wv < 2;
#pragma unroll
  for (int pi = 0; pi < 2; ++pi) {
    const int pr = (pi == 0 || !second) ? wv : wv + 8;
#pragma unroll
    for (int t2 = 0; t2 < 2; ++t2)
#pragma unroll
      for (int ks = 0; ks < 2; ++ks) {
        const int mm = 2 * (pr & 1) + t2;
        w2f[pi][t2][ks] = tr_frag(L + L_W + (9 + (pr >> 1)) * 8192 + (32 * ks + 8 * g4 + tq) * 128 +
                                  (((2 * mm + (tp >> 1)) ^ ((tq >> 1) | ((g4 & 1) << 1) | (ks << 2))) << 4) + (tp & 1) * 8);
      }
  }
  unsigned a_hn = (unsigned)(size_t)(L + L_BFR + (lane >> 3) * 1024 + (wv * 8 + ((lane & 7) ^ (wv & 2))) * 16);
  unsigned a_he = (unsigned)(size_t)(L + L_BFR + 8 * 1024 + (wv * 8 + (((lane & 31) >> 2) ^ (wv & 2))) * 16 + (lane & 3) * 4);
  unsigned a_b1 = (unsigned)(size_t)(L + L_BFR + fr_r);
  unsigned a_h1w = (unsigned)(size_t)(L + ((lane < 32 && n16 < 8) ? L_H1 + n16 * 128 + wv * 16 + g4 * 8 : L_PART + lane * 8));
  unsigned a_h1r = (unsigned)(size_t)(L + L_H1 + (n16 & 7) * 128 + g4 * 16);
  int po = WS_P + ((80 * fh + 8 * wv + 4 * (n16 >> 3) + g4) * 72 + ff) * 128 + (n16 & 7) * 16;
  float emask = lane < 32 ? 1.0f : 0.0f;
  PIN(a_hn); PIN(a_he); PIN(a_b1); PIN(a_h1w); PIN(a_h1r); PIN(po); PIN(emask);
#pragma unroll
  for (int kb = 0; kb < 9; ++kb) PIN(w1f[kb]);
#pragma unroll
  for (int i = 0; i < 8; ++i) PIN(w2f[i >> 2][(i >> 1) & 1][i & 1]);
  if (wv == 7) {
    const v2u tg = *(const LAS v2u*)(L + L_CTL + 16);
    SPIN {
      const v4u r0 = LD128(ws, WS_READY2 + lane * 16, 0, 16), r1 = LD128(ws, WS_READY2 + 1024 + (lane & 7) * 16, 0, 16);
      const unsigned bad = ((r0.x ^ tg.x) | (r0.z ^ tg.x) | (r1.x ^ tg.x) | (r1.z ^ tg.x)) | ((r0.y ^ tg.y) | (r0.w ^ tg.y) | (r1.y ^ tg.y) | (r1.w ^ tg.y));
      if (__all(bad == 0u)) break;
      __builtin_amdgcn_s_sleep(4);
      CBAR();
    }
    CBAR();
  }
  v4u hv; unsigned he;
  {
    const int ho = WS_H + (xg % NREP) * 9216 + wv * 1152;
    SPIN {
      hv = LD128(ws, ho + lane * 16, 0, AUX_SC1); he = LD32(ws, ho + 1024 + (lane & 31) * 4, 0, AUX_SC1);
      if (__all(umax(mx(hv), he) != SENT)) break;
      CBAR();
    }
    STAMP(9);
    if (wv >= 2) { ts[14] = __builtin_amdgcn_s_memtime(); SB(); }
    ln_row(hv, he, emask, a_hn, a_he);
  }
  if (wv >= 2) STAMP(1);
  wg_barrier();
  if (wv >= 2) STAMP(2);
  {
    v4u bfr[9];
#pragma unroll
    for (int kb = 0; kb < 9; ++kb) bfr[kb] = *(const LAS v4u*)(size_t)(a_b1 + kb * 1024);
    SB();
    f32x4 acc = {0.f, 0.f, 0.f, 0.f};
#pragma unroll
    for (int kb = 0; kb < 9; ++kb) acc = mfma(w1f[kb], bfr[kb], acc);
    acc = fold_half4(acc);
    const v2s zero = {0, 0};
    const v2s z0 = __builtin_elementwise_max(__builtin_bit_cast(v2s, pk(acc[0], acc[1])), zero), z1 = __builtin_elementwise_max(__builtin_bit_cast(v2s, pk(acc[2], acc[3])), zero);
    *(LAS v2u*)(size_t)a_h1w = v2u{__builtin_bit_cast(unsigned, z0), __builtin_bit_cast(unsigned, z1)};
    *(LAS v4u*)(L + L_HROW + wv * 1152 + lane * 16) = hv; *(LAS unsigned*)(L + L_HROW + wv * 1152 + 1024 + (lane & 31) * 4) = he;
  }
  if (wv >= 2) STAMP(3);
  wg_barrier();
  if (wv >= 2) STAMP(4);
  {
    const v4u b0 = *(const LAS v4u*)(size_t)a_h1r, b1 = *(const LAS v4u*)(size_t)(a_h1r + 64);
#pragma unroll
    for (int pi = 0; pi < 2; ++pi) {
      if (pi == 0 || second) {
        const f32x4 z4 = {0.f, 0.f, 0.f, 0.f};
        const f32x4 da = mfma(w2f[pi][0][1], b1, mfma(w2f[pi][0][0], b0, z4));
        const f32x4 db = mfma(w2f[pi][1][1], b1, mfma(w2f[pi][1][0], b0, z4));
        v4u ov;
#pragma unroll
        for (int rr = 0; rr < 4; ++rr) ov[rr] = nosentf(n16 < 8 ? da[rr] : db[rr]);
        ST128(ov, ws, po, pi * 64 * 72 * 128);
      }
    }
  }
  STAMP(10);
  if (wv >= 2) { ts[15] = __builtin_amdgcn_s_memtime(); SB(); }
  if (wv != 2) EXIT();
  {
    const int fo = WS_P + (xg * 18 + fe) * 9216 + lane * 16;
    v2u hr = *(const LAS v2u*)(L + L_HROW + (lane & 7) * 1152 + (xg * 18 + fe) * 8);
    int oo = lane < 8 ? lane * 1152 + (xg * 18 + fe) * 8 : 8 * DM * 2;
    PIN(hr); PIN(oo);
    v4u pv[9];
    SPIN {
      unsigned mm = 0;
#pragma unroll
      for (int i = 0; i < 9; ++i) pv[i] = LD128(ws, fo, i * 1024, AUX_SC1);
#pragma unroll
      for (int i = 0; i < 9; ++i) mm = umax(mm, mx(pv[i]));
      if (__all(mm != SENT)) break;
      CBAR();
    }
    STAMP(11);
    f32x4 s = __builtin_bit_cast(f32x4, pv[0]);
#pragma unroll
    for (int i = 1; i < 9; ++i) s += __builtin_bit_cast(f32x4, pv[i]);
#pragma unroll
    for (int d = 0; d < 4; ++d) s[d] += dpp<ROR8>(s[d]);
#pragma unroll
    for (int d = 0; d < 4; ++d) s[d] = x16_sum(s[d]);
#pragma unroll
    for (int d = 0; d < 4; ++d) s[d] = x32_sum(s[d]);
    const v2u ov = {pk2(up2(hr.x) + up2(pk(s[0], s[1]))), pk2(up2(hr.y) + up2(pk(s[2], s[3])))};
    __builtin_amdgcn_raw_buffer_store_b64(ov, mkrs(a.out, 8 * DM * 2), oo, 0, 0);
    STAMP(12);
  }
  EXIT();
}

#include <cstdio>
#include <vector>
#include <algorithm>
typedef unsigned hv4 __attribute__((ext_vector_type(4)));
__global__ void k_fill(hv4* p, size_t n, unsigned v) {
  size_t i = (size_t)blockIdx.x * blockDim.x + threadIdx.x;
  for (; i < n; i += (size_t)gridDim.x * blockDim.x) p[i] = hv4{v, v, v, v};
}
static void dump(const char* what, void* d_ws, hipStream_t stream) {
  std::vector<unsigned long long> hs(256 * 8 * 16);
  (void)hipStreamSynchronize(stream);
  (void)hipMemcpy(hs.data(), (char*)d_ws + WS_ST, hs.size() * 8, hipMemcpyDeviceToHost);
  unsigned long long t0 = ~0ull; for (int i = 0; i < 2048; ++i) t0 = std::min(t0, hs[i * 16]);
  fprintf(stderr, "%s:", what);
  auto st = [&](const char* nm, int j, int lo, int hi, int w0, int w1) { std::vector<double> v; for (int b = lo * 8; b < hi * 8; ++b) for (int w = w0; w < w1; ++w) { unsigned long long t = hs[(b * 8 + w) * 16 + j]; if (t && t >= t0 && t - t0 < 100000000ull) v.push_back((t - t0) * 0.01); } if (v.empty()) return; std::sort(v.begin(), v.end()); fprintf(stderr, " %s %.1f/%.1f/%.1f", nm, v[0], v[v.size() / 2], v.back()); };
  st("s0", 0, 0, 32, 0, 8); st("k0", 9, 27, 32, 0, 8); st("rdy", 1, 0, 32, 0, 1); st("q", 4, 27, 32, 2, 3); st("pub", 7, 0, 32, 0, 8); st("o8", 8, 0, 9, 0, 8); st("o9", 9, 0, 9, 0, 8); st("o11", 11, 0, 9, 0, 1); st("f8", 8, 9, 27, 0, 8); st("f9", 9, 9, 27, 0, 8); st("f10", 10, 9, 27, 0, 8); st("f12", 12, 9, 27, 0, 8);
  fprintf(stderr, "\n");
}
extern "C" void kernel_launch(void* const* d_in, const int* in_sizes, int n_in, void* d_out, int out_size, void* d_ws, size_t ws_size,
                              hipStream_t stream) {
  Args a;
  a.x = (const u16*)d_in[0]; a.kc = (const u16*)d_in[1]; a.vc = (const u16*)d_in[2]; a.wq = (const u16*)d_in[3]; a.wo = (const u16*)d_in[4];
  a.w1 = (const u16*)d_in[6]; a.w2 = (const u16*)d_in[7]; a.out = (u16*)d_out; a.ws = (unsigned char*)d_ws;
  (void)hipFuncSetAttribute((const void*)fused_decode_block, hipFuncAttributeMaxDynamicSharedMemorySize, L_END);
  hipStreamCaptureStatus cs; (void)hipStreamIsCapturing(stream, &cs);
  fprintf(stderr, "ptrs cap=%d x %p k %p v %p wq %p wo %p w1 %p w2 %p out %p ws %p\n", (int)cs, d_in[0], d_in[1], d_in[2], d_in[3], d_in[4], d_in[6], d_in[7], d_out, d_ws);
  hipLaunchKernelGGL(fused_decode_block, dim3(256), dim3(512), L_END, stream, a);
  if (cs == hipStreamCaptureStatusNone) {
    dump("first", d_ws, stream);
    for (int it = 0; it < 3; ++it) { hipLaunchKernelGGL(fused_decode_block, dim3(256), dim3(512), L_END, stream, a); dump("warm", d_ws, stream); }
    for (int it = 0; it < 4; ++it) { k_fill<<<2048, 256, 0, stream>>>((hv4*)d_ws, ws_size / 16, 0); (void)hipStreamSynchronize(stream); hipLaunchKernelGGL(fused_decode_block, dim3(256), dim3(512), L_END, stream, a); dump("cold", d_ws, stream); }
  }
}
```

```cpp
#define FIRST k_first<<<1, 64, 0, stream>>>(tt)
#define LAST k_last<<<1, 64, 0, stream>>>(tt)
#include <hip/hip_runtime.h>
#include <hip/hip_bf16.h>
#include <stdint.h>

typedef unsigned v4u __attribute__((ext_vector_type(4)));
typedef float f32x4 __attribute__((ext_vector_type(4)));
typedef short bf16x8 __attribute__((ext_vector_type(8)));
typedef unsigned short u16;

constexpr int B = 8, D = 576, HQ = 32, HKV = 16, SK = 8192, DH = 128, W = 2048, DFF = 4608;
#define EMU_SCORE 1

__device__ __forceinline__ float bf2f(u16 b) { return __builtin_bit_cast(float, (unsigned)b << 16); }
__device__ __forceinline__ u16 f2bf(float f) { __bf16 h = (__bf16)f; return __builtin_bit_cast(u16, h); }
__device__ __forceinline__ float rbf(float f) { return bf2f(f2bf(f)); }
__device__ __forceinline__ f32x4 mfma(v4u a, v4u b, f32x4 c) {
  return __builtin_amdgcn_mfma_f32_16x16x32_bf16(__builtin_bit_cast(bf16x8, a), __builtin_bit_cast(bf16x8, b), c, 0, 0, 0);
}
__device__ __forceinline__ float wave_sum(float v) {
#pragma unroll
  for (int m = 32; m >= 1; m >>= 1) v += __shfl_xor(v, m);
  return v;
}

__device__ __forceinline__ void ln_row(const u16* x, float* out, int lane) {
  float v[9];
  float s = 0.f;
#pragma unroll
  for (int i = 0; i < 9; ++i) { v[i] = bf2f(x[lane + 64 * i]); s += v[i]; }
  s = wave_sum(s);
  const float mean = s * (1.0f / D);
  float q = 0.f;
#pragma unroll
  for (int i = 0; i < 9; ++i) { float d = v[i] - mean; q += d * d; }
  q = wave_sum(q);
  const float var = rbf(q * (1.0f / D));
  const float mb = rbf(mean);
  const float den = rbf(sqrtf(rbf(var + 1e-5f)));
#pragma unroll
  for (int i = 0; i < 9; ++i) out[lane + 64 * i] = rbf(rbf(v[i] - mb) / den);
}

__global__ void __launch_bounds__(512) k_qproj(const u16* x, const u16* wq, u16* q) {
  __shared__ float xn[B][D];
  __shared__ float red[8][B][64];
  const int lane = threadIdx.x & 63, w = threadIdx.x >> 6;
  ln_row(x + w * D, xn[w], lane);
  __syncthreads();
  const int col = blockIdx.x * 64 + lane;
  float acc[B];
#pragma unroll
  for (int b = 0; b < B; ++b) acc[b] = 0.f;
  for (int k = w * 72; k < w * 72 + 72; ++k) {
    float wv = bf2f(wq[(size_t)k * (HQ * DH) + col]);
#pragma unroll
    for (int b = 0; b < B; ++b) acc[b] += xn[b][k] * wv;
  }
#pragma unroll
  for (int b = 0; b < B; ++b) red[w][b][lane] = acc[b];
  __syncthreads();
  float s = 0.f;
#pragma unroll
  for (int i = 0; i < 8; ++i) s += red[i][w][lane];
  q[w * (HQ * DH) + col] = f2bf(s);
}

__global__ void __launch_bounds__(512, 2) k_attn(const u16* kc, const u16* vc, const u16* q, float* part) {
  __shared__ float sm_o[8][2][DH];
  __shared__ float sm_ml[8][2][2];
  const int lane = threadIdx.x & 63, w = __builtin_amdgcn_readfirstlane(threadIdx.x >> 6);
  const int bid = blockIdx.x;
  const int s = bid & 1, kvh = (bid >> 1) & 15, b = bid >> 5;
  const int g = lane >> 4, c = lane & 15, h = (lane >> 3) & 1;
  const size_t base = ((size_t)(b * HKV + kvh) * SK + (SK - W) + s * 1024 + w * 128) * DH;
  const char* kp = (const char*)(kc + base) + c * 256 + g * 16;
  const char* vp = (const char*)(vc + base) + g * 1024 + c * 16;
  v4u qf[4];
#pragma unroll
  for (int t = 0; t < 4; ++t) qf[t] = *(const v4u*)(q + b * (HQ * DH) + (kvh * 2 + h) * DH + 32 * t + 8 * g);
  v4u kr[8][4], vr[8][4];
#pragma unroll
  for (int i = 0; i < 8; ++i)
#pragma unroll
    for (int t = 0; t < 4; ++t) kr[i][t] = __builtin_nontemporal_load((const v4u*)(kp + i * 4096 + t * 64));
#pragma unroll
  for (int i = 0; i < 3; ++i)
#pragma unroll
    for (int u = 0; u < 4; ++u) vr[i][u] = __builtin_nontemporal_load((const v4u*)(vp + i * 4096 + u * 256));
  __builtin_amdgcn_sched_barrier(0);
  f32x4 sc[8];
#pragma unroll
  for (int i = 0; i < 8; ++i) {
    f32x4 a = {0.f, 0.f, 0.f, 0.f};
#pragma unroll
    for (int t = 0; t < 4; ++t) a = mfma(kr[i][t], qf[t], a);
    sc[i] = a;
    if (i + 3 < 8) {
#pragma unroll
      for (int u = 0; u < 4; ++u) vr[i + 3][u] = __builtin_nontemporal_load((const v4u*)(vp + (i + 3) * 4096 + u * 256));
    }
    __builtin_amdgcn_sched_barrier(0);
  }
  const float scale = 0.08838834764831845f;
  float m = -INFINITY;
#pragma unroll
  for (int i = 0; i < 8; ++i)
#pragma unroll
    for (int r = 0; r < 4; ++r) {
      float v = rbf(rbf(sc[i][r]) * scale);
      sc[i][r] = v; m = fmaxf(m, v);
    }
  m = fmaxf(m, __shfl_xor(m, 16));
  m = fmaxf(m, __shfl_xor(m, 32));
  const float L2E = 1.4426950408889634f;
  float l = 0.f;
  unsigned msk[4];
#pragma unroll
  for (int d = 0; d < 4; ++d) msk[d] = (((lane & 7) >> 1) == d) ? ((lane & 1) ? 0xFFFF0000u : 0x0000FFFFu) : 0u;
  f32x4 o = {0.f, 0.f, 0.f, 0.f};
#pragma unroll
  for (int i = 0; i < 8; ++i) {
#pragma unroll
    for (int u = 0; u < 4; ++u) {
      float p = __builtin_amdgcn_exp2f((sc[i][u] - m) * L2E);
      l += p;
      unsigned pb = f2bf(p); pb |= pb << 16;
      v4u bp = {pb & msk[0], pb & msk[1], pb & msk[2], pb & msk[3]};
      o = mfma(vr[i][u], bp, o);
    }
  }
  l += __shfl_xor(l, 16);
  l += __shfl_xor(l, 32);
#pragma unroll
  for (int r = 0; r < 4; ++r) sm_o[w][h][8 * (4 * g + r) + (lane & 7)] = o[r];
  if (lane == 0 || lane == 8) { sm_ml[w][h][0] = m; sm_ml[w][h][1] = l; }
  __syncthreads();
  if (threadIdx.x < 256) {
    const int hh = threadIdx.x >> 7, d = threadIdx.x & 127;
    float M = -INFINITY;
#pragma unroll
    for (int i = 0; i < 8; ++i) M = fmaxf(M, sm_ml[i][hh][0]);
    float L = 0.f, acc = 0.f;
#pragma unroll
    for (int i = 0; i < 8; ++i) { float wt = __builtin_amdgcn_exp2f((sm_ml[i][hh][0] - M) * L2E); L += wt * sm_ml[i][hh][1]; acc += wt * sm_o[i][hh][d]; }
    float* pp = part + (size_t)((b * HQ + kvh * 2 + hh) * 2 + s) * 132;
    pp[4 + d] = acc;
    if (d == 0) { pp[0] = M; pp[1] = L; }
  }
}

__global__ void __launch_bounds__(512) k_oproj(const float* part, const u16* wo, const u16* x, u16* hout) {
  __shared__ u16 o[B][HQ * DH];
  __shared__ float red[64][B][8];
  const float L2E = 1.4426950408889634f;
  for (int i = threadIdx.x; i < B * HQ * DH; i += 512) {
    const int bh = i >> 7, d = i & 127;
    const float* p0 = part + (size_t)(bh * 2) * 132; const float* p1 = p0 + 132;
    const float M = fmaxf(p0[0], p1[0]);
    const float w0 = __builtin_amdgcn_exp2f((p0[0] - M) * L2E), w1 = __builtin_amdgcn_exp2f((p1[0] - M) * L2E);
    const float L = w0 * p0[1] + w1 * p1[1];
    (&o[0][0])[i] = f2bf((w0 * p0[4 + d] + w1 * p1[4 + d]) / L);
  }
  __syncthreads();
  const int cc = threadIdx.x & 7, kg = threadIdx.x >> 3;
  const int col = blockIdx.x * 8 + cc;
  float acc[B];
#pragma unroll
  for (int b = 0; b < B; ++b) acc[b] = 0.f;
  for (int k = kg; k < HQ * DH; k += 64) {
    float wv = bf2f(wo[(size_t)k * D + col]);
#pragma unroll
    for (int b = 0; b < B; ++b) acc[b] += bf2f(o[b][k]) * wv;
  }
#pragma unroll
  for (int b = 0; b < B; ++b) red[kg][b][cc] = acc[b];
  __syncthreads();
  if (threadIdx.x < 64) {
    const int b = threadIdx.x >> 3;
    float s = 0.f;
    for (int i = 0; i < 64; ++i) s += red[i][b][cc];
    hout[b * D + col] = f2bf(bf2f(x[b * D + col]) + rbf(s));
  }
}

__global__ void __launch_bounds__(512) k_ffn1(const u16* hin, const u16* w1, u16* h1) {
  __shared__ float xn[B][D];
  __shared__ float red[8][B][64];
  const int lane = threadIdx.x & 63, w = threadIdx.x >> 6;
  ln_row(hin + w * D, xn[w], lane);
  __syncthreads();
  const int col = blockIdx.x * 64 + lane;
  float acc[B];
#pragma unroll
  for (int b = 0; b < B; ++b) acc[b] = 0.f;
  for (int k = w * 72; k < w * 72 + 72; ++k) {
    float wv = bf2f(w1[(size_t)k * DFF + col]);
#pragma unroll
    for (int b = 0; b < B; ++b) acc[b] += xn[b][k] * wv;
  }
#pragma unroll
  for (int b = 0; b < B; ++b) red[w][b][lane] = acc[b];
  __syncthreads();
  float s = 0.f;
#pragma unroll
  for (int i = 0; i < 8; ++i) s += red[i][w][lane];
  h1[w * DFF + col] = f2bf(fmaxf(rbf(s), 0.f));
}

__global__ void __launch_bounds__(512) k_ffn2(const u16* h1, const u16* w2, const u16* hin, u16* out) {
  __shared__ float red[64][B][8];
  const int cc = threadIdx.x & 7, kg = threadIdx.x >> 3;
  const int col = blockIdx.x * 8 + cc;
  float acc[B];
#pragma unroll
  for (int b = 0; b < B; ++b) acc[b] = 0.f;
  for (int k = kg; k < DFF; k += 64) {
    float wv = bf2f(w2[(size_t)k * D + col]);
#pragma unroll
    for (int b = 0; b < B; ++b) acc[b] += bf2f(h1[b * DFF + k]) * wv;
  }
#pragma unroll
  for (int b = 0; b < B; ++b) red[kg][b][cc] = acc[b];
  __syncthreads();
  if (threadIdx.x < 64) {
    const int b = threadIdx.x >> 3;
    float s = 0.f;
    for (int i = 0; i < 64; ++i) s += red[i][b][cc];
    out[b * D + col] = f2bf(bf2f(hin[b * D + col]) + rbf(s));
  }
}

__global__ void k_first(unsigned long long* t) { if (blockIdx.x == 0 && threadIdx.x == 0) *t = __builtin_amdgcn_s_memrealtime(); }
__global__ void k_last(const unsigned long long* t) { const unsigned long long e = *t + 25000; while (__builtin_amdgcn_s_memrealtime() < e) __builtin_amdgcn_s_sleep(1); }
extern "C" void kernel_launch(void* const* d_in, const int* in_sizes, int n_in,
                              void* d_out, int out_size, void* d_ws, size_t ws_size,
                              hipStream_t stream) {
  const u16* x = (const u16*)d_in[0];
  const u16* kc = (const u16*)d_in[1];
  const u16* vc = (const u16*)d_in[2];
  const u16* wq = (const u16*)d_in[3];
  const u16* wo = (const u16*)d_in[4];
  const u16* w1 = (const u16*)d_in[6];
  const u16* w2 = (const u16*)d_in[7];
  char* ws = (char*)d_ws;
  u16* q = (u16*)ws;
  float* part = (float*)(ws + 65536);
  u16* h = (u16*)(ws + 65536 + 524288);
  u16* h1 = (u16*)(ws + 65536 + 524288 + 65536);
  unsigned long long* tt = (unsigned long long*)(ws + (8 << 20));
  FIRST;
  k_qproj<<<64, 512, 0, stream>>>(x, wq, q);
  k_attn<<<256, 512, 0, stream>>>(kc, vc, q, part);
  k_oproj<<<72, 512, 0, stream>>>(part, wo, x, h);
  k_ffn1<<<72, 512, 0, stream>>>(h, w1, h1);
  k_ffn2<<<72, 512, 0, stream>>>(h1, w2, h, (u16*)d_out);
  LAST;
}
```

```cpp
#include <hip/hip_runtime.h>
#include <stdint.h>

typedef unsigned v4u __attribute__((ext_vector_type(4)));
typedef unsigned v2u __attribute__((ext_vector_type(2)));
typedef int v4i __attribute__((ext_vector_type(4)));
typedef float f32x4 __attribute__((ext_vector_type(4)));
typedef float f32x2 __attribute__((ext_vector_type(2)));
typedef short bf16x8 __attribute__((ext_vector_type(8)));
typedef short v4s __attribute__((ext_vector_type(4)));
typedef short v2s __attribute__((ext_vector_type(2)));
typedef __bf16 bf16x2 __attribute__((ext_vector_type(2)));
typedef unsigned long long u64;
typedef u64 v8q __attribute__((ext_vector_type(8)));
typedef unsigned short u16;
typedef __amdgpu_buffer_rsrc_t rsrc_t;
#define LAS __attribute__((address_space(3)))

#define CODE_BYTES 24700
#define TSTREAM 1883
#define D0 0
#define FPROF 103, 103, 102, 100, 100, 99, 99, 99
#define QCOST 7
#define BURST 14
#define CFAST 29
#define RB 45
#define GAIN 30
#define DLO 50
#define OBLK 3
#define FB3 2
#define TAUO 10
#define TAUW 18
#define W0M 0x2A
#define W0NM 0x80
#define W3M 0x2
#define W0E 0x4400
#define UCAP 13
#define DL_R 12
#define DL_P 13
#define SL_H 3
#define NREP 8
#define ABITS 8, 10, 13, 15, 11, 12, 14, 9, 16, 17
#define ESTEP 28
#define EOFF 14
#define W2M 0x80
#define NLAST 2
#define DL_O 12

constexpr int DM = 576, HD = 4096, SK = 8192, DH = 128, WND = 2048, DFF = 4608;
constexpr unsigned SENT = 0xFFFFFFFFu;
constexpr int NT = 2, SC1 = 16, SC1NT = 18;
constexpr unsigned SPIN_CAP = 1u << 17;
constexpr int NREPS = 4;
constexpr int FREL[8] = {FPROF};
constexpr int FSUM = 16 * (FREL[0] + FREL[1] + FREL[2] + FREL[3] + FREL[4] + FREL[5] + FREL[6] + FREL[7]) + QCOST * FREL[0] - FREL[7];
constexpr unsigned TAU(int T) { return (unsigned)((TSTREAM - D0) * 16 * FREL[T] / FSUM); }

constexpr int WS_E = 0;
constexpr int WS_TS = 2048;
constexpr int WS_READY = 4096;
constexpr int WS_READY2 = 6144;
constexpr int WS_QP = 8192;
constexpr int WS_O = WS_QP + 3 * 8 * 4096 * 4;
constexpr int WS_R = WS_O + 256 * 640;
constexpr int WS_H = WS_R + 9 * 8 * 8 * 256;
constexpr int WS_P = WS_H + NREP * 9216;
constexpr int WS_END = WS_P + 144 * 72 * 128;

constexpr int C_Q = 0, C_ARR = 4, C_ISS = 8, C_TAGOK = 12, C_TAG = 16, C_PUB = 24, C_CERT = 28, C_T0 = 32, C_ML = 36, C_PACE = 40, C_WS = 56, C_WA = 60;
constexpr int L_Q = 64;
constexpr int L_ML = 640;
constexpr int L_H1 = 768;
constexpr int L_XR = 1792;
constexpr int L_PART = 2048;
constexpr int L_BFR = L_PART + 8192;
constexpr int L_HROW = L_BFR + 9216;
constexpr int L_W = L_HROW + 9216;
constexpr int L_END = L_W + 14 * 8192;
static_assert(L_W % 128 == 0 && L_END <= 160 * 1024 && L_END > 80 * 1024, "LDS: one workgroup per CU");

struct Args { const u16 *x, *kc, *vc, *wq, *wo, *w1, *w2; unsigned char* ws; u16* out; };

__device__ __forceinline__ rsrc_t mkrs(const void* p, int bytes) { return __builtin_amdgcn_make_buffer_rsrc((void*)p, 0, bytes, 0x00020000); }
__device__ __forceinline__ v4i mkrs4(const void* p, int bytes) { return v4i{(int)(size_t)p, (int)((size_t)p >> 32) & 0xFFFF, bytes, 0x00020000}; }
__device__ __forceinline__ float bflo(unsigned u) { return __builtin_bit_cast(float, u << 16); }
__device__ __forceinline__ float bfhi(unsigned u) { return __builtin_bit_cast(float, u & 0xFFFF0000u); }
__device__ __forceinline__ f32x2 up2(unsigned u) { return f32x2{bflo(u), bfhi(u)}; }
__device__ __forceinline__ unsigned pk(float lo, float hi) {
  const bf16x2 v = {(__bf16)lo, (__bf16)hi};
  unsigned r = __builtin_bit_cast(unsigned, v);
  asm("" : "+v"(r));
  return r;
}
__device__ __forceinline__ unsigned pk2(f32x2 v) { return pk(v[0], v[1]); }
__device__ __forceinline__ float rbf(float f) { return bflo(pk(f, f)); }
__device__ __forceinline__ unsigned nosent(unsigned u) { return u < 0xFFFFFFFEu ? u : 0xFFFFFFFEu; }
__device__ __forceinline__ unsigned nosentf(float f) { return nosent(__builtin_bit_cast(unsigned, f)); }
__device__ __forceinline__ f32x4 mfma(v4u a, v4u b, f32x4 c) {
  return __builtin_amdgcn_mfma_f32_16x16x32_bf16(__builtin_bit_cast(bf16x8, a), __builtin_bit_cast(bf16x8, b), c, 0, 0, 0);
}
template <int CTRL> __device__ __forceinline__ float dpp(float x) {
  return __builtin_bit_cast(float, __builtin_amdgcn_mov_dpp(__builtin_bit_cast(int, x), CTRL, 0xf, 0xf, true));
}
constexpr int XOR1 = 0xB1, XOR2 = 0x4E, HMIR = 0x141, ROR8 = 0x128;
__device__ __forceinline__ float x16_sum(float x) {
  auto s = __builtin_amdgcn_permlane16_swap(__float_as_uint(x), __float_as_uint(x), false, false);
  return __uint_as_float(s[0]) + __uint_as_float(s[1]);
}
__device__ __forceinline__ float x32_sum(float x) {
  auto s = __builtin_amdgcn_permlane32_swap(__float_as_uint(x), __float_as_uint(x), false, false);
  return __uint_as_float(s[0]) + __uint_as_float(s[1]);
}
__device__ __forceinline__ float x16_max(float x) {
  auto s = __builtin_amdgcn_permlane16_swap(__float_as_uint(x), __float_as_uint(x), false, false);
  return fmaxf(__uint_as_float(s[0]), __uint_as_float(s[1]));
}
__device__ __forceinline__ float x32_max(float x) {
  auto s = __builtin_amdgcn_permlane32_swap(__float_as_uint(x), __float_as_uint(x), false, false);
  return fmaxf(__uint_as_float(s[0]), __uint_as_float(s[1]));
}
__device__ __forceinline__ float sum_b012(float x) { x += dpp<XOR1>(x); x += dpp<XOR2>(x); return x + dpp<HMIR>(x); }
__device__ __forceinline__ float sum_b345(float x) { x += dpp<ROR8>(x); return x32_sum(x16_sum(x)); }
__device__ __forceinline__ float ln_inv(float var) { return __builtin_amdgcn_rcpf(rbf(__builtin_amdgcn_sqrtf(rbf(rbf(var) + 1e-5f)))); }
__device__ __forceinline__ unsigned ln_norm(f32x2 v, float mb, float inv) { return pk2(up2(pk2(v - mb)) * inv); }
__device__ __forceinline__ unsigned umax(unsigned a, unsigned b) { return a > b ? a : b; }
__device__ __forceinline__ unsigned mx(v4u v) { return umax(umax(v.x, v.y), umax(v.z, v.w)); }
#define LD128(rs, vo, so, aux) __builtin_amdgcn_raw_buffer_load_b128(rs, vo, so, aux)
#define LD64(rs, vo, so, aux) __builtin_amdgcn_raw_buffer_load_b64(rs, vo, so, aux)
#define LD32(rs, vo, so, aux) __builtin_amdgcn_raw_buffer_load_b32(rs, vo, so, aux)
#define ST128(v, rs, vo, aux) __builtin_amdgcn_raw_buffer_store_b128(v, rs, vo, 0, aux)
#define ST64(v, rs, vo, aux) __builtin_amdgcn_raw_buffer_store_b64(v, rs, vo, 0, aux)
#define ST32(v, rs, vo, aux) __builtin_amdgcn_raw_buffer_store_b32(v, rs, vo, 0, aux)
#define CBAR() asm volatile("" ::: "memory")
#define PIN(x) asm volatile("" : "+v"(x))
#define SB() __builtin_amdgcn_sched_barrier(0)
#define DRAIN() asm volatile("s_waitcnt vmcnt(0)" ::: "memory")
#define LDSWAIT() asm volatile("s_waitcnt lgkmcnt(0)" ::: "memory")
#define SPIN _Pragma("nounroll") for (unsigned spins = 0; spins < SPIN_CAP; ++spins)
#define NOW() ((unsigned)__builtin_amdgcn_s_memrealtime())
__device__ __forceinline__ void wg_barrier() { LDSWAIT(); __builtin_amdgcn_s_barrier(); CBAR(); }

__device__ __forceinline__ void lds_wait(LAS unsigned char* p, unsigned want) {
  unsigned f;
  asm volatile("1:\n\tds_read_b32 %0, %1\n\ts_waitcnt lgkmcnt(0)\n\tv_cmp_le_u32 vcc, %2, %0\n\ts_cbranch_vccnz 2f\n\ts_sleep 1\n\ts_branch 1b\n2:"
               : "=&v"(f) : "v"((unsigned)(size_t)p), "s"(want) : "vcc", "memory");
}
__device__ __forceinline__ void lds_inc(LAS unsigned char* p) {
  asm volatile("s_mov_b64 exec, 1\n\tds_add_u32 %0, %1\n\ts_mov_b64 exec, -1" :: "v"((unsigned)(size_t)p), "v"(1u) : "memory");
}
__device__ __forceinline__ unsigned take_slot(unsigned ctr, unsigned vtau, unsigned t0) {
  unsigned v, w, x, s;
  asm volatile("ds_read_b32 %1, %4 offset:8\n\tds_read_b32 %2, %4 offset:12\n\ts_mov_b64 exec, 1\n\ts_waitcnt lgkmcnt(0)\n\ts_memrealtime vcc\n\t"
               "v_mul_u32_u24 %1, %1, %5\n\tv_lshrrev_b32 %1, 8, %1\n\tds_add_rtn_u32 %0, %4, %1\n\tv_mul_u32_u24 %2, %2, %1\n\tv_lshrrev_b32 %2, 5, %2\n\t"
               "s_waitcnt lgkmcnt(0)\n\ts_sub_u32 %3, vcc_lo, %6\n\ts_lshl_b32 %3, %3, 4\n\tv_max_u32 %0, %3, %0\n\t"
               "ds_max_u32 %4, %0 offset:4\n\tds_add_rtn_u32 %1, %4, %2 offset:4\n\ts_mov_b64 exec, -1\n\ts_waitcnt lgkmcnt(0)\n\tv_readfirstlane_b32 %3, %1\n\ts_nop 0\n\t"
               "s_lshr_b32 %3, %3, 4\n\ts_add_u32 %3, %3, %6\n\ts_sub_u32 %3, %3, %7"
               : "=&v"(v), "=&v"(w), "=&v"(x), "=&s"(s) : "v"(ctr), "v"(vtau), "s"(t0), "n"(BURST) : "vcc", "scc", "memory");
  return s;
}
__device__ __forceinline__ void wait_until(unsigned t) {
  asm volatile("1:\n\ts_memrealtime vcc\n\ts_waitcnt lgkmcnt(0)\n\ts_sub_u32 vcc_lo, vcc_lo, %0\n\ts_cmp_lt_i32 vcc_lo, 0\n\ts_cbranch_scc0 2f\n\ts_sleep 1\n\ts_branch 1b\n2:"
               :: "s"(t) : "vcc", "scc", "memory");
}
__device__ __forceinline__ void pace(unsigned ctr, unsigned vtau, unsigned t0) { wait_until(take_slot(ctr, vtau, t0)); }
template <int PITCH> __device__ __forceinline__ void load_block(v4i rs, int soff, unsigned dst, int lane) {
  static_assert(PITCH % 128 == 0, "the chunk swizzle is applied to the byte offset");
  const int v0 = (lane >> 3) * PITCH + (((lane & 7) ^ ((lane >> 4) & 1)) << 4);
  unsigned keep;
#define LB_(v) "s_nop 0\n\tbuffer_load_dwordx4 " v ", %7, %1 offen nt lds\n\ts_add_u32 m0, m0, 0x400\n\ts_add_u32 %1, %1, %8\n\t"
  asm volatile("s_nop 4\n\ts_mov_b32 %0, m0\n\ts_mov_b32 m0, %2\n\t" LB_("%3") LB_("%4") LB_("%3") LB_("%4") LB_("%5") LB_("%6") LB_("%5") LB_("%6") "s_mov_b32 m0, %0"
               : "=&s"(keep), "+s"(soff) : "s"(dst), "v"(v0), "v"(v0 ^ 32), "v"(v0 ^ 64), "v"(v0 ^ 96), "s"(rs), "n"(8 * PITCH) : "scc", "memory");
#undef LB_
}
__device__ __forceinline__ v4u tr_frag(LAS unsigned char* p) {
  const v4s lo = __builtin_amdgcn_ds_read_tr16_b64_v4i16((LAS v4s*)p);
  const v4s hi = __builtin_amdgcn_ds_read_tr16_b64_v4i16((LAS v4s*)(p + 512));
  const v2u l = __builtin_bit_cast(v2u, lo), h = __builtin_bit_cast(v2u, hi);
  return v4u{l.x, l.y, h.x, h.y};
}
__device__ __forceinline__ f32x4 fold_half4(f32x4 x) {
  f32x4 t;
#pragma unroll
  for (int i = 0; i < 4; ++i) { auto s = __builtin_amdgcn_permlane32_swap(__float_as_uint(x[i]), __float_as_uint(x[i]), false, false); x[i] = __uint_as_float(s[0]); t[i] = __uint_as_float(s[1]); }
#pragma unroll
  for (int i = 0; i < 4; ++i) x[i] += dpp<ROR8>(t[i]);
  return x;
}
__device__ __forceinline__ void ln_row(v4u hv, unsigned he, float emask, unsigned a_hn, unsigned a_he) {
  f32x2 v[5];
#pragma unroll
  for (int d = 0; d < 4; ++d) v[d] = up2(hv[d]);
  v[4] = up2(he);
  const float piv = __builtin_bit_cast(float, __builtin_amdgcn_readfirstlane(__builtin_bit_cast(int, v[0][0])));
  f32x2 d[5];
#pragma unroll
  for (int i = 0; i < 5; ++i) d[i] = v[i] - piv;
  d[4] *= emask;
  const f32x2 t1 = ((d[0] + d[1]) + (d[2] + d[3])) + d[4];
  const f32x2 t2 = (d[0] * d[0] + d[1] * d[1]) + (d[2] * d[2] + (d[3] * d[3] + d[4] * d[4]));
  float s1 = t1[0] + t1[1], s2 = t2[0] + t2[1];
  s1 += dpp<XOR1>(s1); s2 += dpp<XOR1>(s2);
  s1 += dpp<XOR2>(s1); s2 += dpp<XOR2>(s2);
  s1 += dpp<HMIR>(s1); s2 += dpp<HMIR>(s2);
  s1 += dpp<ROR8>(s1); s2 += dpp<ROR8>(s2);
  auto sw = __builtin_amdgcn_permlane16_swap(__float_as_uint(s1), __float_as_uint(s2), false, false);
  float c = __uint_as_float(sw[0]) + __uint_as_float(sw[1]);
  c = x32_sum(c);
  s1 = __builtin_bit_cast(float, __builtin_amdgcn_readlane(__builtin_bit_cast(int, c), 0)) * (1.0f / DM);
  s2 = __builtin_bit_cast(float, __builtin_amdgcn_readlane(__builtin_bit_cast(int, c), 16)) * (1.0f / DM);
  const float mb = rbf(piv + s1);
  unsigned cc[5];
#pragma unroll
  for (int i = 0; i < 5; ++i) cc[i] = pk2(v[i] - mb);
  const float inv = ln_inv(fmaxf(fmaf(-s1, s1, s2), 0.f));
#pragma unroll
  for (int i = 0; i < 5; ++i) v[i] = up2(cc[i]);
#pragma unroll
  for (int i = 0; i < 5; ++i) cc[i] = pk2(v[i] * inv);
  *(LAS v4u*)(size_t)a_hn = v4u{cc[0], cc[1], cc[2], cc[3]};
  *(LAS unsigned*)(size_t)a_he = cc[4];
}

__global__ void __launch_bounds__(512, 2) fused_decode_block(Args args_in_kernarg_segment) {
  v8q ka;
  asm volatile("s_load_dwordx16 %0, %1, 0x0" : "=&s"(ka) : "s"(__builtin_amdgcn_kernarg_segment_ptr()));
  SB();
  extern __shared__ __attribute__((aligned(128))) unsigned char smem[];
  LAS unsigned char* L = (LAS unsigned char*)smem;
  const int lane = threadIdx.x & 63, wv = __builtin_amdgcn_readfirstlane(threadIdx.x >> 6);
  const int bid = blockIdx.x, xg = bid & 7, r = bid >> 3;
  unsigned warm0, warm1;
  {
    u64 pc;
    asm volatile("s_getpc_b64 %0" : "=s"(pc));
    warm0 = LD32(mkrs((const void*)pc, CODE_BYTES), 0, (wv * 32 + r) * 128, 0);
  }
  const int kvh = 2 * xg + (r & 1), sp = (r >> 1) & 1, bb = r >> 2;
  const int n16 = lane & 15, g4 = lane >> 4;
  constexpr int AB[10] = {ABITS};
  const int wbyte = ((wv & 1) << AB[4]) + (((wv >> 1) & 1) << AB[5]) + ((wv >> 2) << AB[6]);
#define UOFF(T, t) ((((t) & 1) << AB[2]) + (((t) >> 1) << AB[3]) + (((T) & 1) << AB[7]) + ((((T) >> 1) & 1) << AB[8]) + (((T) >> 2) << AB[9]))
#define LDKV(rs, vo, T, t) LD128(rs, (vo) + (UOFF(T, t) & 4095), UOFF(T, t) & ~4095, NT)
  size_t kvoff = ((size_t)(bb * 16 + kvh) * SK + (SK - WND) + sp * 1024) * (DH * 2) + wbyte;
  int ko = (((n16 >> 2) & 1) << AB[0]) + ((n16 >> 3) << AB[1]) + g4 * 64 + (lane & 3) * 16;
  const int vo = ((g4 & 1) << AB[0]) + ((g4 >> 1) << AB[1]) + n16 * 16;
  asm volatile("" : "+s"(kvoff));
  PIN(ko);
  SB();
  u64 tm;
  asm volatile("s_memrealtime %1\n\ts_waitcnt lgkmcnt(0)" : "+s"(ka), "=&s"(tm));
  SB();
  Args a;
  a.x = (const u16*)ka[0]; a.kc = (const u16*)ka[1]; a.vc = (const u16*)ka[2]; a.wq = (const u16*)ka[3]; a.wo = (const u16*)ka[4];
  a.w1 = (const u16*)ka[5]; a.w2 = (const u16*)ka[6]; a.ws = (unsigned char*)ka[7];
  const unsigned t_my = (unsigned)tm;
  const rsrc_t rk = mkrs((const char*)a.kc + kvoff, 1024 * 256 - wbyte);
  v4u kr[8][4], vr[8][4];
  if (wv < 2) {
#pragma unroll
    for (int t = 0; t < 4; ++t) kr[0][t] = LDKV(rk, ko, 0, t);
  }
  SB();
  if (wv == 7) {
    LAS v4u* cw = (LAS v4u*)L;
    cw[0] = v4u{0u, 0u, 0u, 0u}; cw[1] = v4u{0u, 0u, 0u, 0u};
    cw[2] = v4u{t_my, 0u, D0 * 16 + (8 + QCOST) * TAU(0), 0u}; cw[3] = v4u{256u, 32u, 0u, 0u};
  }
  wg_barrier();
  if (wv >= 2) {
    wait_until(t_my + D0 - BURST + ((wv * TAU(0)) >> 4));
    SB();
#pragma unroll
    for (int t = 0; t < 4; ++t) kr[0][t] = LDKV(rk, ko, 0, t);
  }
  SB();
  warm1 = LD32(mkrs((const char*)a.vc + kvoff, wv == 0 ? 4 : 0), 0, 0, NT);
  const rsrc_t rv = mkrs((const char*)a.vc + kvoff, 1024 * 256 - wbyte);
  const int xo = xg ^ 4;
  const bool role_o = r < 9, role_f = r >= 9 && r < 27, role_q = r >= 8;
  const int fe = r - 9, ff = 9 * xg + (fe >> 1), fh = fe & 1, fu = xg * 18 + fe;
  const int fpre = (role_f && (ff & 7) == 3) ? FB3 : 0;
  const int fr_w = (lane ^ ((lane >> 3) & 2)) * 16;
  const int fr_r = ((n16 & 7) * 8 + (((n16 >> 3) * 4 + g4) ^ (n16 & 2))) * 16;
  const rsrc_t ws = mkrs(a.ws, WS_END);
  const int orec_my = WS_O + ((kvh * 8 + bb) * 2 + sp) * 640;
  const float L2E = 1.4426950408889634f;
  const v4u S4 = {SENT, SENT, SENT, SENT};
  unsigned pctr = (unsigned)(size_t)(L + C_PACE);
  u64 tag = 0;
  v2u xres = {0u, 0u};
  if (wv == 1) {
    tag = __builtin_bit_cast(u64, LD64(ws, WS_E, 0, SC1));
    xres = LD64(mkrs(a.x, role_o ? 8 * DM * 2 : 0), (xg * DM + 64 * r + 4 * n16) * 2, 0, 0);
    SB();
    if (lane < 40) ST128(S4, ws, orec_my + lane * 16, SC1NT);
    if (role_o) {
      const int o = WS_R + ((r * 8 + g4) * 8 + xg) * 256 + n16 * 16;
      ST128(S4, ws, o, SC1NT); ST128(S4, ws, o + 4 * 8 * 256, SC1NT);
#pragma unroll
      for (int i = 0; i < NREP / 4; ++i) ST64((v2u{SENT, SENT}), ws, WS_H + (g4 + 4 * i) * 9216 + xg * 1152 + r * 128 + n16 * 8, SC1NT);
    }
  }
  SB();
  const unsigned t_start = __builtin_amdgcn_readfirstlane(*(volatile LAS unsigned*)(L + C_T0));
  if (wv == 2) {
    DRAIN();
    const unsigned t0w = D0 * 16 + 8 * TAU(0), tl = (NOW() - t_start + RB) * 16 - 2 * TAU(0), tw = tl > t0w ? tl : t0w;
    if (lane == 0) {
      __hip_atomic_fetch_max((LAS unsigned*)(L + C_PACE + 4), tw + QCOST * TAU(0), __ATOMIC_RELAXED, __HIP_MEMORY_SCOPE_WORKGROUP);
      *(volatile LAS unsigned*)(L + C_WS) = tw >> 4;
    }
  }
  auto weight_slot = [&]() { lds_wait(L + C_WS, 1u); return __builtin_amdgcn_readfirstlane(*(volatile LAS unsigned*)(L + C_WS)) + t_start - BURST; };
  if (__builtin_expect(wv == 0, 0)) {
    __builtin_amdgcn_s_setprio(3);
    if (role_q) {
      const int qp = xg * 24 + r - 8, qj = qp & 63, qs = qp >> 6;
      v4u xr[9], wr[3][8];
      const rsrc_t rq = mkrs(a.wq, DM * HD * 2), rx = mkrs(a.x, 8 * DM * 2);
      const int wl = (32 * (n16 >> 3) + 8 * g4) * (HD * 2) + (n16 & 7) * 16;
#pragma unroll
      for (int i = 0; i < 9; ++i) { int kb = 3 * qs + i; kb = kb >= 9 ? kb - 9 : kb; xr[i] = LD128(rx, (lane >> 3) * (DM * 2) + (lane & 7) * 16, kb * 128, 0); }
      SB();
      wait_until(weight_slot());
      SB();
#pragma unroll
      for (int i = 0; i < 3; ++i)
#pragma unroll
        for (int t = 0; t < 8; ++t) {
          if ((t & 3) == 0 && (i + t) > 0) { SB(); __builtin_amdgcn_s_sleep(4); SB(); }
          wr[i][t] = LD128(rq, wl, qj * 128 + (192 * qs + 64 * i + t) * (HD * 2), NT);
        }
      SB();
      f32x2 s = {0.f, 0.f}, s_ = {0.f, 0.f};
#pragma unroll
      for (int i = 0; i < 9; ++i) { s += up2(xr[i][0]); s_ += up2(xr[i][1]); s += up2(xr[i][2]); s_ += up2(xr[i][3]); }
      s += s_;
      const float mean = sum_b012(s[0] + s[1]) * (1.0f / DM);
      f32x2 q = {0.f, 0.f}, q_ = {0.f, 0.f};
#pragma unroll
      for (int i = 0; i < 9; ++i)
#pragma unroll
        for (int d = 0; d < 4; d += 2) { const f32x2 u = up2(xr[i][d]) - mean, v = up2(xr[i][d + 1]) - mean; q += u * u; q_ += v * v; }
      q += q_;
      const float inv = ln_inv(sum_b012(q[0] + q[1]) * (1.0f / DM)), mb = rbf(mean);
#pragma unroll
      for (int i = 0; i < 3; ++i)
        *(LAS v4u*)(L + L_BFR + i * 1024 + fr_w) = v4u{ln_norm(up2(xr[i][0]), mb, inv), ln_norm(up2(xr[i][1]), mb, inv), ln_norm(up2(xr[i][2]), mb, inv), ln_norm(up2(xr[i][3]), mb, inv)};
      LDSWAIT();
      f32x4 acc[8];
#pragma unroll
      for (int i = 0; i < 3; ++i) {
        const v4u bf = *(const LAS v4u*)(L + L_BFR + i * 1024 + fr_r);
#pragma unroll
        for (int j = 0; j < 8; ++j) {
          v4u af;
#pragma unroll
          for (int d = 0; d < 4; ++d) af[d] = __builtin_amdgcn_perm(wr[i][2 * d + 1][j >> 1], wr[i][2 * d][j >> 1], (j & 1) ? 0x07060302u : 0x05040100u);
          acc[j] = mfma(af, bf, i ? acc[j] : f32x4{0.f, 0.f, 0.f, 0.f});
        }
      }
      if ((g4 >> 1) == (n16 >> 3)) {
        LAS f32x4* red = (LAS f32x4*)(L + L_HROW + (((g4 >> 1) * 8 + (n16 & 7)) * 68 + 32 * (g4 & 1)) * 4);
#pragma unroll
        for (int rr = 0; rr < 4; ++rr) {
          red[2 * rr] = f32x4{acc[0][rr], acc[1][rr], acc[2][rr], acc[3][rr]};
          red[2 * rr + 1] = f32x4{acc[4][rr], acc[5][rr], acc[6][rr], acc[7][rr]};
        }
      }
      LDSWAIT();
      const int qo = WS_QP + (((g4 * 16 + (qj >> 2)) * 3 + qs) * 256 + 64 * (qj & 3) + 4 * n16) * 4;
#pragma unroll
      for (int i = 0; i < 2; ++i) {
        const LAS f32x4* red = (const LAS f32x4*)(L + L_HROW) + (4 * i + g4) * 17 + n16;
        ST128(__builtin_bit_cast(v4u, red[0] + red[8 * 17]), ws, qo + i * 4 * 16 * 3 * 256 * 4, SC1NT);
      }
    }
    DRAIN();
    lds_wait(L + C_TAGOK, 1u);
    if (lane == 0) ST64(*(volatile LAS v2u*)(L + C_TAG), ws, WS_READY + (role_q ? xg * 24 + r - 8 : 192 + xg * 8 + r) * 8, SC1);
  } else if (wv == 1) {
    DRAIN();
    *(LAS v2u*)(L + L_XR + n16 * 8) = xres;
    tag += 1;
    if (lane == 0) { *(volatile LAS u64*)(L + C_TAG) = tag; *(volatile LAS unsigned*)(L + C_TAGOK) = 1u; }
  }
  __builtin_amdgcn_s_setprio(2);

  f32x4 o_acc = {0.f, 0.f, 0.f, 0.f};
  float c0 = 0.f;
  auto final_ml = [&](int h, float& M, float& inv) {
    f32x4 ml[4];
#pragma unroll
    for (int w = 0; w < 4; ++w) ml[w] = *(const LAS f32x4*)(L + L_ML + h * 64 + w * 16);
    M = fmaxf(ml[0][0], ml[0][2]);
#pragma unroll
    for (int w = 1; w < 4; ++w) M = fmaxf(M, fmaxf(ml[w][0], ml[w][2]));
    const float e0 = __builtin_amdgcn_exp2f((ml[0][0] - M) * L2E);
    float Ls = fmaf(e0, ml[0][1], __builtin_amdgcn_exp2f((ml[0][2] - M) * L2E) * ml[0][3]);
#pragma unroll
    for (int w = 1; w < 4; ++w) Ls = fmaf(__builtin_amdgcn_exp2f((ml[w][2] - M) * L2E), ml[w][3], fmaf(__builtin_amdgcn_exp2f((ml[w][0] - M) * L2E), ml[w][1], Ls));
    inv = __builtin_amdgcn_rcpf(Ls);
    c0 = e0 * inv;
  };
  unsigned a_pw = (unsigned)(size_t)(L + L_PART + (wv * 256 + (n16 >> 3) * 128 + 32 * g4 + (lane & 7)) * 4), a_pr = (unsigned)(size_t)(L + L_PART + lane * 16);
  int o_off = orec_my + lane * 8;
  {
    const bool late = wv == 0 && role_q, early_w = wv == 3 && (role_o || fpre > 0);
    if (early_w) {
      unsigned tw0 = weight_slot() + (role_q ? EOFF : 0);
      for (int kb = 0; kb < (role_o ? OBLK : fpre); ++kb) {
        wait_until(tw0);
        tw0 += ESTEP;
        if (role_o) load_block<DM * 2>(mkrs4(a.wo, HD * DM * 2), (512 * xo + 64 * kb) * (DM * 2) + r * 128, (unsigned)(size_t)(L + L_W + kb * 8192), lane);
        else load_block<DFF * 2>(mkrs4(a.w1, DM * DFF * 2), 64 * kb * (DFF * 2) + ff * 128, (unsigned)(size_t)(L + L_W + kb * 8192), lane);
      }
    }
#define LOADU(U) do { _Pragma("unroll") for (int t = 0; t < 4; ++t) { \
      if ((U) < 8) kr[(U) & 7][t] = LDKV(rk, ko, (U) & 7, t); else vr[(U) & 7][t] = LDKV(rv, vo, (U) & 7, t); } } while (0)
#define PAIR(U) do { const bool d_ = (late && ((W0M >> (U)) & 1)) || (wv == 0 && !late && ((W0NM >> (U)) & 1)) || (early_w && ((W3M >> (U)) & 1)) || (wv == 2 && ((W2M >> (U)) & 1)); \
    SB(); DRAIN(); pace(pctr, d_ ? 2 * TAU((U) >> 1) : TAU((U) >> 1), t_start); SB(); LOADU(U); \
    SB(); if (!d_) { DRAIN(); pace(pctr, TAU(((U) + 1) >> 1), t_start); } SB(); LOADU((U) + 1); SB(); } while (0)
    PAIR(1);
    if (wv == 1 && r >= 32 - NREPS) {
      SB();
      const v2u sw = *(volatile LAS v2u*)(L + C_PACE);
      const unsigned lag = sw.y > sw.x ? (sw.y - sw.x) >> 4 : 0u;
      if (lane == 0) ST64(__builtin_bit_cast(v2u, (tag << 16) | ((t_start + lag) & 0xFFFFu)), ws, WS_TS + ((31 - r) * 8 + xg) * 8, SC1);
      SB();
    }
    PAIR(3);
    if (wv == 1) {
      SB();
      const u64 e = __builtin_bit_cast(u64, LD64(ws, WS_TS + lane * 8, 0, SC1));
      const bool ok = lane < 8 * NREPS && (e >> 16) == (tag & 0xFFFFFFFFFFFFull);
      float d = ok ? (float)(short)((unsigned)e - t_start) : 0.f, n = ok ? 1.f : 0.f;
      d = sum_b345(d); n = sum_b345(n);
      float dx = n > 0.f ? d * __builtin_amdgcn_rcpf(n) : -1000.f;
      dx = fmaxf(dx, dpp<XOR1>(dx)); dx = fmaxf(dx, dpp<XOR2>(dx)); dx = fmaxf(dx, dpp<HMIR>(dx));
      d = sum_b012(d); n = sum_b012(n);
      const v2u sw = *(volatile LAS v2u*)(L + C_PACE);
      const unsigned lag = sw.y > sw.x ? sw.y - sw.x : 0u;
      d = fmaxf(d * __builtin_amdgcn_rcpf(fmaxf(n, 1.f)), dx - (float)GAIN) - (float)lag * (1.f / 16.f);
      d = fminf(fmaxf(d, -(float)DLO), 200.f);
      const float left = fmaxf((float)TSTREAM - (float)sw.x * (1.f / 16.f), 800.f);
      if (lane == 0) {
        __hip_atomic_fetch_add((LAS unsigned*)(L + C_PACE), lag, __ATOMIC_RELAXED, __HIP_MEMORY_SCOPE_WORKGROUP);
        *(volatile LAS v2u*)(L + C_PACE + 8) = v2u{(unsigned)(256.5f + 256.f * d * __builtin_amdgcn_rcpf(left)), CFAST};
      }
      SB();
    }
    PAIR(5);
    if (wv == 2) {
      SB();
      __builtin_amdgcn_s_setprio(3);
      lds_wait(L + C_TAGOK, 1u);
      const v2u tg = *(const LAS v2u*)(L + C_TAG);
      const int li = lane < 12 ? lane : 0;
      SPIN {
        const v2u r0 = LD64(ws, WS_READY + ((li >> 2) * 64 + 4 * kvh + (li & 3)) * 8, 0, SC1);
        if (__all(((r0.x ^ tg.x) | (r0.y ^ tg.y)) == 0u)) break;
        __builtin_amdgcn_s_sleep(20);
        CBAR();
      }
      CBAR();
      const int go = WS_QP + (bb * 16 + kvh) * 3072 + lane * 16;
      const f32x4 q0 = __builtin_bit_cast(f32x4, LD128(ws, go, 0, SC1NT)), q1 = __builtin_bit_cast(f32x4, LD128(ws, go + 1024, 0, SC1NT)),
                  q2 = __builtin_bit_cast(f32x4, LD128(ws, go + 2048, 0, SC1NT));
      const f32x4 qq = (q0 + q1) + q2;
      *(LAS v2u*)(L + L_Q + lane * 8) = v2u{pk(qq[0], qq[1]), pk(qq[2], qq[3])};
      LDSWAIT();
      if (lane == 0) *(volatile LAS unsigned*)(L + C_Q) = 1u;
      __builtin_amdgcn_s_setprio(2);
      SB();
    }
    PAIR(7);
    SB(); DRAIN(); pace(pctr, TAU(4), t_start); SB(); LOADU(9); SB();
    lds_wait(L + C_Q, 1u);
    SB();
    asm volatile("" :: "v"(warm0), "v"(warm1));
    const v4u qf = *(const LAS v4u*)(L + L_Q + (n16 >> 3) * 256 + g4 * 64 + (lane & 3) * 16);
    unsigned msk[4];
#pragma unroll
    for (int d = 0; d < 4; ++d) msk[d] = (((lane & 7) >> 1) == d) ? ((lane & 1) ? 0xFFFF0000u : 0x0000FFFFu) : 0u;
    const float scale = 0.08838834764831845f;
    auto scores = [&](const v4u (&k)[4], unsigned (&sc)[2]) {
      f32x4 s, dd[4];
#pragma unroll
      for (int t = 0; t < 4; ++t) dd[t] = mfma(k[t], qf, f32x4{0.f, 0.f, 0.f, 0.f});
#pragma unroll
      for (int t = 0; t < 4; ++t) s[t] = (dpp<0x00>(dd[t][0]) + dpp<0x55>(dd[t][1])) + (dpp<0xAA>(dd[t][2]) + dpp<0xFF>(dd[t][3]));
      sc[0] = pk2(up2(pk(s[0], s[1])) * scale); sc[1] = pk2(up2(pk(s[2], s[3])) * scale);
    };
    auto smax = [&](const unsigned (&sc)[2], float m) { return fmaxf(fmaxf(fmaxf(bflo(sc[0]), bfhi(sc[0])), m), fmaxf(bflo(sc[1]), bfhi(sc[1]))); };
    auto pfrag = [&](float p) { const unsigned pb = pk(p, p); return v4u{pb & msk[0], pb & msk[1], pb & msk[2], pb & msk[3]}; };
#define UNIT(U) do { SB(); if ((U) <= UCAP && wv != 0) DRAIN(); wait_until(__builtin_amdgcn_readfirstlane(due)); SB(); LOADU(U); SB(); \
    if ((U) < 15) { due = take_slot(pctr, TAU(((U) + 1) >> 1), t_start); if (wv == 0 && ((W0E >> (U)) & 1)) due = t_start; } SB(); } while (0)
    unsigned due = take_slot(pctr, TAU(5), t_start);
    SB();
    unsigned sc[8][2];
    float m1 = -INFINITY;
#pragma unroll
    for (int T = 0; T < 4; ++T) { scores(kr[T], sc[T]); m1 = smax(sc[T], m1); }
    UNIT(10);
#pragma unroll
    for (int T = 4; T < 8; ++T) { scores(kr[T], sc[T]); m1 = smax(sc[T], m1); }
    m1 = x32_max(x16_max(m1));
    UNIT(11);
    float pl[32], l1 = 0.f;
#pragma unroll
    for (int u = 0; u < 32; ++u) { pl[u] = __builtin_amdgcn_exp2f((((u & 1) ? bfhi(sc[u >> 2][(u >> 1) & 1]) : bflo(sc[u >> 2][(u >> 1) & 1])) - m1) * L2E); l1 += pl[u]; }
    l1 = x32_sum(x16_sum(l1));
    if ((lane & 55) == 0) *(LAS f32x2*)(L + L_ML + ((lane >> 3) * 8 + wv) * 8) = f32x2{m1, l1};
    lds_inc(L + C_ML);
    UNIT(12);
    UNIT(13);
    float c2 = 1.0f;
    if (wv != 0) {
      lds_wait(L + C_ML, 8u);
      float M, inv;
      final_ml(n16 >> 3, M, inv);
      c2 = __builtin_amdgcn_exp2f((m1 - M) * L2E) * inv;
      if (wv == 1) ST32(nosentf(fmaf(M, L2E, -__builtin_amdgcn_logf(inv))), ws, lane < 32 ? orec_my + 512 + ((n16 >> 3) * 16 + g4 * 8 + (lane & 7)) * 4 : WS_END, SC1);
      c0 = __builtin_bit_cast(float, __builtin_amdgcn_ds_bpermute((lane >> 5) * 32, __builtin_bit_cast(int, c0)));
    }
#define PV(T) do { _Pragma("unroll") for (int u = 0; u < 4; ++u) o_acc = mfma(vr[T][u], pfrag(pl[4 * (T) + u] * c2), o_acc); } while (0)
    PV(0); PV(1); PV(2);
    UNIT(14);
    PV(3); PV(4); PV(5);
    UNIT(15);
    lds_inc(L + C_ISS);
    v4u pf[8];
#pragma unroll
    for (int u = 0; u < 8; ++u) { pf[u] = pfrag(pl[24 + u] * c2); PIN(pf[u]); }
    PIN(c0); PIN(a_pw); PIN(a_pr); PIN(o_off);
    SB();
#pragma unroll
    for (int u = 0; u < 8; ++u) o_acc = mfma(vr[6 + (u >> 2)][u & 3], pf[u], o_acc);
  }
#pragma unroll
  for (int rr = 0; rr < 4; ++rr) *(LAS float*)(size_t)(a_pw + 32 * rr) = o_acc[rr];
  CBAR();
  unsigned arr;
  {
    f32x4 ov[8];
    unsigned tk;
    asm volatile("s_mov_b64 exec, 1\n\tds_add_rtn_u32 %0, %9, %10\n\ts_mov_b64 exec, -1\n\t"
                 "ds_read_b128 %1, %11\n\tds_read_b128 %2, %11 offset:1024\n\tds_read_b128 %3, %11 offset:2048\n\tds_read_b128 %4, %11 offset:3072\n\t"
                 "ds_read_b128 %5, %11 offset:4096\n\tds_read_b128 %6, %11 offset:5120\n\tds_read_b128 %7, %11 offset:6144\n\tds_read_b128 %8, %11 offset:7168\n\ts_waitcnt lgkmcnt(0)"
                 : "=&v"(tk), "=&v"(ov[0]), "=&v"(ov[1]), "=&v"(ov[2]), "=&v"(ov[3]), "=&v"(ov[4]), "=&v"(ov[5]), "=&v"(ov[6]), "=&v"(ov[7])
                 : "v"((unsigned)(size_t)(L + C_ARR)), "v"(1u), "v"(a_pr) : "memory");
    arr = __builtin_amdgcn_readfirstlane(tk);
    if (arr == 7) {
      if (wv == 0) { float M, inv; final_ml(lane >> 5, M, inv); }
      const f32x4 o0 = {fmaf(ov[0][0], c0, ov[1][0]), fmaf(ov[0][1], c0, ov[1][1]), fmaf(ov[0][2], c0, ov[1][2]), fmaf(ov[0][3], c0, ov[1][3])};
      const f32x4 o = (o0 + (ov[2] + ov[3])) + ((ov[4] + ov[5]) + (ov[6] + ov[7]));
      ST64((v2u{nosent(pk(o[0], o[1])), nosent(pk(o[2], o[3]))}), ws, o_off, SC1);
      CBAR();
      if (lane == 0) *(volatile LAS unsigned*)(L + C_PUB) = 1u;
    }
  }
  if (!role_o && !role_f) return;

  const v2u tg = *(const LAS v2u*)(L + C_TAG);
  const int ob = 2 * (wv & 3) + (lane >> 5);
  const int orec = WS_O + ((2 * xo + (wv >> 2)) * 8 + ob) * 1280;
  const int oo = orec + (lane & 31) * 16, lo = orec + 512 + (g4 & 1) * 64 + (lane & 1) * 640;
  if (arr == 0) {
    SPIN {
      const v4u r0 = LD128(ws, WS_READY + lane * 16, 0, SC1), r1 = LD128(ws, WS_READY + 1024 + lane * 16, 0, SC1);
      const unsigned bad = ((r0.x ^ tg.x) | (r0.z ^ tg.x) | (r1.x ^ tg.x) | (r1.z ^ tg.x)) | ((r0.y ^ tg.y) | (r0.w ^ tg.y) | (r1.y ^ tg.y) | (r1.w ^ tg.y));
      if (__all(bad == 0u)) break;
      __builtin_amdgcn_s_sleep(8);
      CBAR();
    }
    CBAR();
    if (lane == 0) { *(volatile LAS unsigned*)(L + C_CERT) = 1u; if (bid == 0) ST64(tg, ws, WS_E, SC1); }
    __builtin_amdgcn_s_setprio(0);
    const v4i ra = mkrs4(role_o ? a.wo : a.w2, role_o ? HD * DM * 2 : DFF * DM * 2), r1 = mkrs4(a.w1, DM * DFF * 2);
    lds_wait(L + C_ISS, 8u);
    unsigned tw = NOW();
    for (int kb = role_o ? OBLK : fpre; kb < (role_o ? 8 : 14 - fh); ++kb) {
      wait_until(tw);
      tw += role_o ? TAUO : TAUW;
      if (role_f && kb < 9) load_block<DFF * 2>(r1, 64 * kb * (DFF * 2) + ff * 128, (unsigned)(size_t)(L + L_W + kb * 8192), lane);
      else load_block<DM * 2>(ra, role_o ? (512 * xo + 64 * kb) * (DM * 2) + r * 128 : 64 * ff * (DM * 2) + (5 * fh + kb - 9) * 128, (unsigned)(size_t)(L + L_W + kb * 8192), lane);
    }
    if (role_o) { asm volatile("s_waitcnt vmcnt(%0)" :: "n"(8 * NLAST) : "memory"); lds_inc(L + C_WA); }
  } else if (arr == 4 && role_f) {
    lds_wait(L + C_PUB, 1u);
#pragma unroll
    for (int i = 0; i < 9; ++i) ST128(S4, ws, WS_P + fu * 9216 + i * 1024 + lane * 16, SC1);
    DRAIN();
    if (lane == 0) ST64(tg, ws, WS_READY2 + fu * 8, SC1);
  }
  const int tq = (lane >> 2) & 3, tp = lane & 3;
  const int half_off = (32 * (tp >> 1) + 8 * g4 + tq) * 128 + ((wv ^ ((tq >> 1) | ((g4 & 1) << 1) | ((tp >> 1) << 2))) << 4) + (tp & 1) * 8;

  if (role_o) {
    int so = WS_R + ((r * 8 + 4 * wv + g4) * 8 + xg) * 256 + n16 * 16;
    unsigned lrw = (unsigned)(size_t)(L + ((lane < 32 && n16 < 8) ? L_HROW + n16 * 272 + (8 * wv + 4 * g4) * 4 : L_HROW + 4096 + lane * 16));
    unsigned lrr = (unsigned)(size_t)(L + L_HROW + (4 * wv + g4) * 272 + n16 * 16);
    int ro = WS_R + (r * 8 + xg) * 2048 + lane * 16;
    int ho = WS_H + g4 * 9216 + xg * 1152 + r * 128 + n16 * 8;
    unsigned lbw = (unsigned)(size_t)(L + L_BFR + (4 * (wv >> 2) + ((lane >> 3) & 3)) * 1024 + (ob * 8 + ((lane & 7) ^ (ob & 2))) * 16), lb = (unsigned)(size_t)(L + L_BFR + fr_r);
    PIN(so); PIN(lrw); PIN(lrr); PIN(ro); PIN(ho); PIN(lb); PIN(lbw);
    lds_wait(L + C_CERT, 1u);
    lds_wait(L + C_PUB, 1u);
    if (arr != 0) __builtin_amdgcn_s_sleep(DL_O);
    v4u o0 = LD128(ws, oo, 0, SC1), o1 = LD128(ws, oo + 640, 0, SC1);
    unsigned ls = LD32(ws, lo, 0, SC1);
    SB();
    lds_wait(L + C_WA, 1u);
    v4u wf[8];
#pragma unroll
    for (int kb = 0; kb < 8 - NLAST; ++kb) wf[kb] = tr_frag(L + L_W + kb * 8192 + half_off);
    xres = *(const LAS v2u*)(L + L_XR + n16 * 8);
    SB();
    SPIN {
      if (__all(umax(umax(mx(o0), mx(o1)), ls) != SENT)) break;
      CBAR();
      o0 = LD128(ws, oo, 0, SC1); o1 = LD128(ws, oo + 640, 0, SC1); ls = LD32(ws, lo, 0, SC1);
    }
    {
      const float dl = dpp<0xF5>(__uint_as_float(ls)) - dpp<0xA0>(__uint_as_float(ls));
      const float w0 = __builtin_amdgcn_rcpf(1.0f + __builtin_amdgcn_exp2f(dl)), w1 = __builtin_amdgcn_rcpf(1.0f + __builtin_amdgcn_exp2f(-dl));
      v4u bf;
#pragma unroll
      for (int d = 0; d < 4; ++d) bf[d] = pk2(up2(o0[d]) * w0 + up2(o1[d]) * w1);
      *(LAS v4u*)(size_t)lbw = bf;
    }
    DRAIN();
    wg_barrier();
    v4u bfr[8];
#pragma unroll
    for (int kb = 0; kb < 8; ++kb) bfr[kb] = *(const LAS v4u*)(size_t)(lb + kb * 1024);
#pragma unroll
    for (int kb = 8 - NLAST; kb < 8; ++kb) wf[kb] = tr_frag(L + L_W + kb * 8192 + half_off);
    SB();
    f32x4 acc = {0.f, 0.f, 0.f, 0.f};
#pragma unroll
    for (int kb = 0; kb < 8; ++kb) acc = mfma(wf[kb], bfr[kb], acc);
    *(LAS f32x4*)(size_t)lrw = fold_half4(acc);
    wg_barrier();
    if (wv < 2) { const v4u t = *(const LAS v4u*)(size_t)lrr; ST128((v4u{nosent(t.x), nosent(t.y), nosent(t.z), nosent(t.w)}), ws, so, SC1); }
    if (wv != 2) return;
    v4u p0, p1;
    __builtin_amdgcn_s_sleep(DL_R);
    SPIN {
      p0 = LD128(ws, ro, 0, SC1); p1 = LD128(ws, ro + 1024, 0, SC1);
      if (__all(umax(mx(p0), mx(p1)) != SENT)) break;
      CBAR();
    }
    f32x4 sm = __builtin_bit_cast(f32x4, p0) + __builtin_bit_cast(f32x4, p1);
#pragma unroll
    for (int d = 0; d < 4; ++d) sm[d] = x16_sum(sm[d]);
#pragma unroll
    for (int d = 0; d < 4; ++d) sm[d] = x32_sum(sm[d]);
    const v2u hv = {nosent(pk2(up2(xres.x) + up2(pk(sm[0], sm[1])))), nosent(pk2(up2(xres.y) + up2(pk(sm[2], sm[3]))))};
#pragma unroll
    for (int i = 0; i < NREP / 4; ++i) ST64(hv, ws, ho + 4 * i * 9216, SC1);
    return;
  }

  { u64 po_; asm volatile("s_load_dwordx2 %0, %1, 0x40\n\ts_waitcnt lgkmcnt(0)" : "=&s"(po_) : "s"(__builtin_amdgcn_kernarg_segment_ptr())); a.out = (u16*)po_; }
  DRAIN();
  wg_barrier();
  const int ho = WS_H + (xg % NREP) * 9216 + wv * 1152;
  v4u hv = LD128(ws, ho + lane * 16, 0, SC1);
  unsigned he = LD32(ws, ho + 1024 + (lane & 31) * 4, 0, SC1);
  const int r2o = WS_READY2 + (18 * wv + (lane < 18 ? lane : 0)) * 8;
  v2u r2 = LD64(ws, r2o, 0, SC1);
  SB();
  v4u w1f[9], w2f[2][2][2];
#pragma unroll
  for (int kb = 0; kb < 9; ++kb) w1f[kb] = tr_frag(L + L_W + kb * 8192 + half_off);
  const bool second = fh == 0 && wv < 2;
#pragma unroll
  for (int pi = 0; pi < 2; ++pi) {
    const int pr = (pi == 0 || !second) ? wv : wv + 8;
#pragma unroll
    for (int t2 = 0; t2 < 2; ++t2)
#pragma unroll
      for (int ks = 0; ks < 2; ++ks) {
        const int mm = 2 * (pr & 1) + t2;
        w2f[pi][t2][ks] = tr_frag(L + L_W + (9 + (pr >> 1)) * 8192 + (32 * ks + 8 * g4 + tq) * 128 +
                                  (((2 * mm + (tp >> 1)) ^ ((tq >> 1) | ((g4 & 1) << 1) | (ks << 2))) << 4) + (tp & 1) * 8);
      }
  }
  unsigned a_hn = (unsigned)(size_t)(L + L_BFR + (lane >> 3) * 1024 + (wv * 8 + ((lane & 7) ^ (wv & 2))) * 16);
  unsigned a_he = (unsigned)(size_t)(L + L_BFR + 8 * 1024 + (wv * 8 + (((lane & 31) >> 2) ^ (wv & 2))) * 16 + (lane & 3) * 4);
  unsigned a_b1 = (unsigned)(size_t)(L + L_BFR + fr_r);
  unsigned a_h1w = (unsigned)(size_t)(L + ((lane < 32 && n16 < 8) ? L_H1 + n16 * 128 + wv * 16 + g4 * 8 : L_PART + lane * 8));
  unsigned a_h1r = (unsigned)(size_t)(L + L_H1 + (n16 & 7) * 128 + g4 * 16);
  int po = WS_P + ((80 * fh + 8 * wv + 4 * (n16 >> 3) + g4) * 72 + ff) * 128 + (n16 & 7) * 16;
  int po2 = po + 64 * 72 * 128;
  float emask = lane < 32 ? 1.0f : 0.0f;
  if (second) __builtin_amdgcn_s_setprio(3); else __builtin_amdgcn_s_setprio(1);
  PIN(po2); PIN(a_hn); PIN(a_he); PIN(a_b1); PIN(a_h1w); PIN(a_h1r); PIN(po); PIN(emask);
#pragma unroll
  for (int kb = 0; kb < 9; ++kb) PIN(w1f[kb]);
#pragma unroll
  for (int i = 0; i < 8; ++i) PIN(w2f[i >> 2][(i >> 1) & 1][i & 1]);
  SB();
  SPIN {
    if (__all(((r2.x ^ tg.x) | (r2.y ^ tg.y)) == 0u)) break;
    __builtin_amdgcn_s_sleep(8);
    CBAR();
    r2 = LD64(ws, r2o, 0, SC1);
  }
  CBAR();
  SPIN {
    if (__all(umax(mx(hv), he) != SENT)) break;
    __builtin_amdgcn_s_sleep(SL_H);
    CBAR();
    hv = LD128(ws, ho + lane * 16, 0, SC1); he = LD32(ws, ho + 1024 + (lane & 31) * 4, 0, SC1);
  }
  ln_row(hv, he, emask, a_hn, a_he);
  wg_barrier();
  {
    v4u bfr[9];
#pragma unroll
    for (int kb = 0; kb < 9; ++kb) bfr[kb] = *(const LAS v4u*)(size_t)(a_b1 + kb * 1024);
    SB();
    *(LAS v4u*)(L + L_HROW + wv * 1152 + lane * 16) = hv; *(LAS unsigned*)(L + L_HROW + wv * 1152 + 1024 + (lane & 31) * 4) = he;
    f32x4 acc = {0.f, 0.f, 0.f, 0.f};
#pragma unroll
    for (int kb = 0; kb < 9; ++kb) acc = mfma(w1f[kb], bfr[kb], acc);
    acc = fold_half4(acc);
    const v2s zero = {0, 0};
    const v2s z0 = __builtin_elementwise_max(__builtin_bit_cast(v2s, pk(acc[0], acc[1])), zero), z1 = __builtin_elementwise_max(__builtin_bit_cast(v2s, pk(acc[2], acc[3])), zero);
    *(LAS v2u*)(size_t)a_h1w = v2u{__builtin_bit_cast(unsigned, z0), __builtin_bit_cast(unsigned, z1)};
  }
  wg_barrier();
  {
    const v4u b0 = *(const LAS v4u*)(size_t)a_h1r, b1 = *(const LAS v4u*)(size_t)(a_h1r + 64);
    const f32x4 z4 = {0.f, 0.f, 0.f, 0.f};
    if (second) {
      const f32x4 da = mfma(w2f[0][0][1], b1, mfma(w2f[0][0][0], b0, z4)), db = mfma(w2f[0][1][1], b1, mfma(w2f[0][1][0], b0, z4));
      const f32x4 dc = mfma(w2f[1][0][1], b1, mfma(w2f[1][0][0], b0, z4)), dd = mfma(w2f[1][1][1], b1, mfma(w2f[1][1][0], b0, z4));
      v4u ov, ow;
#pragma unroll
      for (int rr = 0; rr < 4; ++rr) { ov[rr] = nosentf(n16 < 8 ? da[rr] : db[rr]); ow[rr] = nosentf(n16 < 8 ? dc[rr] : dd[rr]); }
      PIN(ov); PIN(ow);
      ST128(ov, ws, po, SC1);
      ST128(ow, ws, po2, SC1);
    } else {
      const f32x4 da = mfma(w2f[0][0][1], b1, mfma(w2f[0][0][0], b0, z4)), db = mfma(w2f[0][1][1], b1, mfma(w2f[0][1][0], b0, z4));
      v4u ov;
#pragma unroll
      for (int rr = 0; rr < 4; ++rr) ov[rr] = nosentf(n16 < 8 ? da[rr] : db[rr]);
      ST128(ov, ws, po, SC1);
    }
  }
  if (wv != 2) return;
  {
    const int fo = WS_P + fu * 9216 + lane * 16;
    v2u hr = *(const LAS v2u*)(L + L_HROW + (lane & 7) * 1152 + fu * 8);
    int oo = lane < 8 ? lane * 1152 + fu * 8 : 8 * DM * 2;
    PIN(hr); PIN(oo);
    v4u pv[9];
    __builtin_amdgcn_s_sleep(DL_P);
    SPIN {
      unsigned mm = 0;
#pragma unroll
      for (int i = 0; i < 9; ++i) pv[i] = LD128(ws, fo, i * 1024, SC1);
#pragma unroll
      for (int i = 0; i < 9; ++i) mm = umax(mm, mx(pv[i]));
      if (__all(mm != SENT)) break;
      CBAR();
    }
    f32x4 s = __builtin_bit_cast(f32x4, pv[0]);
#pragma unroll
    for (int i = 1; i < 9; ++i) s += __builtin_bit_cast(f32x4, pv[i]);
#pragma unroll
    for (int d = 0; d < 4; ++d) s[d] += dpp<ROR8>(s[d]);
#pragma unroll
    for (int d = 0; d < 4; ++d) s[d] = x16_sum(s[d]);
#pragma unroll
    for (int d = 0; d < 4; ++d) s[d] = x32_sum(s[d]);
    const v2u ov = {pk2(up2(hr.x) + up2(pk(s[0], s[1]))), pk2(up2(hr.y) + up2(pk(s[2], s[3])))};
    ST64(ov, mkrs(a.out, 8 * DM * 2), oo, 0);
  }
}

extern "C" void kernel_launch(void* const* d_in, const int* in_sizes, int n_in, void* d_out, int out_size, void* d_ws, size_t ws_size,
                              hipStream_t stream) {
  Args a;
  a.x = (const u16*)d_in[0]; a.kc = (const u16*)d_in[1]; a.vc = (const u16*)d_in[2]; a.wq = (const u16*)d_in[3]; a.wo = (const u16*)d_in[4];
  a.w1 = (const u16*)d_in[6]; a.w2 = (const u16*)d_in[7]; a.ws = (unsigned char*)d_ws; a.out = (u16*)d_out;
  (void)hipFuncSetAttribute((const void*)fused_decode_block, hipFuncAttributeMaxDynamicSharedMemorySize, L_END);
  hipLaunchKernelGGL(fused_decode_block, dim3(256), dim3(512), L_END, stream, a);
}
```
